```python
import torch

def _softmax(x, axis=-1):
    return torch.softmax(x, dim=axis)

def _gelu(x):
    return torch.nn.functional.gelu(x, approximate='tanh')

def _silu(x):
    return torch.nn.functional.silu(x)

def _sigmoid(x):
    return torch.sigmoid(x)

def _layernorm(x, axis=-1, eps=1e-05):
    m = x.mean(dim=axis, keepdim=True)
    v = x.var(dim=axis, keepdim=True, unbiased=False)
    return (x - m) / torch.sqrt(v + eps)

def _scatter_add(tgt, idx, src, axis=0):
    return tgt.clone().index_add_(axis, idx, src)

def _segment_sum(src, seg, n):
    out = torch.zeros((n,) + src.shape[1:], dtype=src.dtype)
    return out.index_add_(0, seg, src)

def _pad(x, pw):
    flat = tuple((v for pair in reversed(pw) for v in pair))
    return torch.nn.functional.pad(x, flat)
kernel_name = 'standalone__dit_block__diffusion'

def setup_inputs():

    _w = lambda a: (lambda t: t.to(torch.bfloat16) if t.is_floating_point() else t)(torch.as_tensor(a))
    B, N, D = (2, 1024, 384)
    F = 4 * D
    return {'x': _w((lambda *s: torch.randn(*s, dtype=torch.bfloat16) * 0.05)(B, N, D)), 'c': _w((lambda *s: torch.randn(*s, dtype=torch.bfloat16) * 0.2)(B, D)), 'wqkv': _w((lambda *s: torch.randn(*s, dtype=torch.bfloat16) * 0.05)(D, 3 * D)), 'wo': _w((lambda *s: torch.randn(*s, dtype=torch.bfloat16) * 0.05)(D, D)), 'w_mod': _w((lambda *s: torch.randn(*s, dtype=torch.bfloat16) * 0.2)(D, 6 * D)), 'w_fc1': _w((lambda *s: torch.randn(*s, dtype=torch.bfloat16) * 0.05)(D, F)), 'w_fc2': _w((lambda *s: torch.randn(*s, dtype=torch.bfloat16) * 0.05)(F, D))}

def reference(x, c, wqkv, wo, w_mod, w_fc1, w_fc2):
    B, N, D = x.shape
    H = 2
    Dh = D // H
    mod = (_silu(c) @ w_mod).reshape(B, 6, 1, D)
    shift_a, scale_a, gate_a, shift_m, scale_m, gate_m = [mod[:, i] for i in range(6)]
    h = _layernorm(x, axis=-1) * (1 + scale_a) + shift_a
    qkv = (h @ wqkv).reshape(B, N, 3, H, Dh)
    q, k, v = (qkv[:, :, 0].clone(), qkv[:, :, 1].clone(), qkv[:, :, 2].clone())
    attn = _softmax(torch.einsum('bqhd,bkhd->bhqk', q, k) * Dh ** (-0.5), axis=-1)
    a = torch.einsum('bhqk,bkhd->bqhd', attn, v).reshape(B, N, D) @ wo
    x = x + gate_a * a
    h2 = _layernorm(x, axis=-1) * (1 + scale_m) + shift_m
    m = _gelu(h2 @ w_fc1) @ w_fc2
    return x + gate_m * m
```

```cpp
#include <hip/hip_runtime.h>
#include <hip/hip_bf16.h>
typedef unsigned short u16;
__device__ __forceinline__ float b2f(u16 v){ return __uint_as_float(((unsigned)v)<<16); }
__device__ __forceinline__ u16 f2b(float f){ unsigned u=__float_as_uint(f); u += 0x7FFFu + ((u>>16)&1u); return (u16)(u>>16); }
constexpr int B=2,N=1024,D=384,F=1536,H=2,DH=192,M=B*N;

__global__ void k_mod(const u16* c,const u16* wm,float* mod){
  int j=blockIdx.x*blockDim.x+threadIdx.x; if(j>=B*6*D) return;
  int b=j/(6*D), col=j%(6*D); float s=0;
  for(int k=0;k<D;k++){ float cv=b2f(c[b*D+k]); float si=cv/(1.f+__expf(-cv)); s+=si*b2f(wm[k*6*D+col]); }
  mod[j]=s;
}
__global__ void k_ln(const u16* xb,const float* xf,const float* mod,int so,int sc,float* h){
  int row=blockIdx.x; int b=row/N; __shared__ float r[2][64];
  int t=threadIdx.x; float v[6]; float s=0;
  for(int i=0;i<6;i++){ int k=t+64*i; v[i]= xb? b2f(xb[row*D+k]) : xf[row*D+k]; s+=v[i]; }
  for(int o=32;o;o>>=1) s+=__shfl_xor(s,o);
  float m=s/D; float q=0; for(int i=0;i<6;i++){ float d=v[i]-m; q+=d*d; }
  for(int o=32;o;o>>=1) q+=__shfl_xor(q,o);
  float rs=rsqrtf(q/D+1e-5f);
  for(int i=0;i<6;i++){ int k=t+64*i; h[row*D+k]=(v[i]-m)*rs*(1.f+mod[b*6*D+sc*D+k])+mod[b*6*D+so*D+k]; }
}
template<int MODE>
__global__ void k_gemm(const float* A,const u16* W,int K,int Nn,float* out,const u16* xb,const float* xf,const float* mod,int gi,u16* outb){
  int col=blockIdx.x*64+threadIdx.x; int row0=blockIdx.y*16;
  float acc[16]; for(int i=0;i<16;i++) acc[i]=0;
  for(int k=0;k<K;k++){ float w=b2f(W[k*Nn+col]);
    #pragma unroll
    for(int i=0;i<16;i++) acc[i]+=A[(row0+i)*K+k]*w; }
  for(int i=0;i<16;i++){ int row=row0+i; int b=row/N; float v=acc[i];
    if(MODE==0) out[row*Nn+col]=v;
    if(MODE==1) out[row*Nn+col]=b2f(xb[row*Nn+col])+mod[b*6*D+gi*D+col]*v;
    if(MODE==2){ float u=0.7978845608f*(v+0.044715f*v*v*v); out[row*Nn+col]=0.5f*v*(1.f+tanhf(u)); }
    if(MODE==3) outb[row*Nn+col]=f2b(xf[row*Nn+col]+mod[b*6*D+gi*D+col]*v);
  }
}
__global__ void k_attn(const float* qkv,float* o){
  int q=blockIdx.x%N; int h=(blockIdx.x/N)%H; int b=blockIdx.x/(N*H);
  __shared__ float p[N]; __shared__ float qs[DH]; __shared__ float red[256];
  int t=threadIdx.x; const float* base=qkv+(size_t)b*N*3*D;
  if(t<DH) qs[t]=base[q*3*D+h*DH+t]; __syncthreads();
  float mx=-1e30f;
  for(int kk=t;kk<N;kk+=256){ float s=0; const float* kr=base+kk*3*D+D+h*DH; for(int d=0;d<DH;d++) s+=qs[d]*kr[d]; s*=rsqrtf((float)DH); p[kk]=s; mx=fmaxf(mx,s);}
  red[t]=mx; __syncthreads(); for(int s=128;s;s>>=1){ if(t<s) red[t]=fmaxf(red[t],red[t+s]); __syncthreads(); }
  mx=red[0]; __syncthreads(); float sm=0;
  for(int kk=t;kk<N;kk+=256){ float e=__expf(p[kk]-mx); p[kk]=e; sm+=e; }
  red[t]=sm; __syncthreads(); for(int s=128;s;s>>=1){ if(t<s) red[t]+=red[t+s]; __syncthreads(); }
  sm=red[0];
  if(t<DH){ float a=0; for(int kk=0;kk<N;kk++) a+=p[kk]*base[kk*3*D+2*D+h*DH+t]; o[((size_t)b*N+q)*D+h*DH+t]=a/sm; }
}
extern "C" void kernel_launch(void* const* d_in,const int* in_sizes,int n_in,void* d_out,int out_size,void* d_ws,size_t ws_size,hipStream_t stream){
  const u16* x=(const u16*)d_in[0]; const u16* c=(const u16*)d_in[1]; const u16* wqkv=(const u16*)d_in[2]; const u16* wo=(const u16*)d_in[3];
  const u16* wm=(const u16*)d_in[4]; const u16* w1=(const u16*)d_in[5]; const u16* w2=(const u16*)d_in[6];
  float* ws=(float*)d_ws; float* mod=ws; float* h=ws+8192; float* qkv=h+M*D; float* o=qkv+M*3*D; float* x1=o+M*D; float* hh=x1+M*D;
  k_mod<<<(B*6*D+63)/64,64,0,stream>>>(c,wm,mod);
  k_ln<<<M,64,0,stream>>>(x,nullptr,mod,0,1,h);
  k_gemm<0><<<dim3(3*D/64,M/16),64,0,stream>>>(h,wqkv,D,3*D,qkv,nullptr,nullptr,mod,0,nullptr);
  k_attn<<<B*H*N,256,0,stream>>>(qkv,o);
  k_gemm<1><<<dim3(D/64,M/16),64,0,stream>>>(o,wo,D,D,x1,x,nullptr,mod,2,nullptr);
  k_ln<<<M,64,0,stream>>>(nullptr,x1,mod,3,4,h);
  k_gemm<2><<<dim3(F/64,M/16),64,0,stream>>>(h,w1,D,F,hh,nullptr,nullptr,mod,0,nullptr);
  k_gemm<3><<<dim3(D/64,M/16),64,0,stream>>>(hh,w2,F,D,nullptr,nullptr,x1,mod,5,(u16*)d_out);
}
```

```cpp
#include <hip/hip_runtime.h>
#include <hip/hip_bf16.h>

typedef unsigned short u16;
typedef unsigned u32;
typedef unsigned long long u64;
typedef u32 v4u __attribute__((ext_vector_type(4)));
typedef u32 v2u __attribute__((ext_vector_type(2)));
typedef float f4 __attribute__((ext_vector_type(4)));
typedef float f2 __attribute__((ext_vector_type(2)));
typedef __bf16 b8 __attribute__((ext_vector_type(8)));
typedef __bf16 b2 __attribute__((ext_vector_type(2)));
typedef short s4 __attribute__((ext_vector_type(4)));
#define LAS __attribute__((address_space(3)))
#define GAS __attribute__((address_space(1)))
#define DEV __device__ __forceinline__
typedef __amdgpu_buffer_rsrc_t rsrc_t;

#define STAMP 0

namespace dit {

constexpr int SC1 = 16;
constexpr u32 SPIN_CAP = 1u << 16;

constexpr int WS_EPOCH = 0;
constexpr int WS_START = 1024;
constexpr int WS_RTF = 4096;
constexpr int WS_FLAG = 8192;
constexpr int WS_MOD = WS_FLAG + 256 * 512;
constexpr int WS_ML = WS_MOD + 2 * 2304 * 8;
constexpr int WS_ST = WS_ML + 256 * 512;
constexpr int WS_UV = WS_ST + 32 * 4096;
constexpr int WS_Q = WS_UV + 2 * 96 * 128;
constexpr int WS_K = WS_Q + 64 * 24576;
constexpr int WS_V = WS_K + 64 * 24576;
constexpr int WS_OP = WS_V + 64 * 24576;
constexpr int WS_X1 = WS_OP + 256 * 24576;
constexpr int WS_HH = WS_X1 + 32 * 49152;
constexpr int WS_WOT = WS_HH + 32 * 196608;
constexpr int WS_W1T = WS_WOT + 24 * 12 * 1024;
constexpr int WS_W2T = WS_W1T + 2 * 96 * 12 * 1024;
constexpr int WS_END = WS_W2T + 96 * 12 * 1024;
constexpr int WS_STAMP = WS_END;
static_assert(WS_Q % 128 == 0 && WS_MOD % 128 == 0 && WS_ML % 128 == 0 && WS_UV % 128 == 0, "line alignment");

constexpr int L_WB = 0;
constexpr int L_Q = 0;
constexpr int L_P = 24576;
constexpr int L_MX = 57344;
constexpr int L_OC = 58368;
constexpr int L_RED = 0;
constexpr int L_ACT = 98304;
constexpr int L_MODA = 147456;
constexpr int L_MODB = 150528;
constexpr int L_GATE = 153600;
constexpr int L_SC = 156672;
constexpr int L_STL = 158208;
constexpr int L_BYTES = 158720;

DEV f4 mfma(v4u a, v4u b, f4 c) {
  return __builtin_amdgcn_mfma_f32_16x16x32_bf16(__builtin_bit_cast(b8, a), __builtin_bit_cast(b8, b), c, 0, 0, 0);
}
DEV float asf(u32 v) { return __builtin_bit_cast(float, v); }
DEV u32 asu(float v) { return __builtin_bit_cast(u32, v); }
DEV u32 pk(float a, float b) { f2 v = {a, b}; return __builtin_bit_cast(u32, __builtin_convertvector(v, b2)); }
DEV float lo(u32 v) { return __builtin_bit_cast(float, v << 16); }
DEV float hi(u32 v) { return __builtin_bit_cast(float, v & 0xffff0000u); }
DEV float rbf(float v) { return lo(pk(v, v)); }
DEV rsrc_t mkrs(const void* p, int bytes) { return __builtin_amdgcn_make_buffer_rsrc((void*)p, 0, bytes, 0x00020000); }
template <int AUX> DEV v4u ld16(rsrc_t r, int v, int s) { return __builtin_amdgcn_raw_buffer_load_b128(r, v, s, AUX); }
template <int AUX> DEV v2u ld8(rsrc_t r, int v, int s) { return __builtin_amdgcn_raw_buffer_load_b64(r, v, s, AUX); }
template <int AUX> DEV void st16(v4u x, rsrc_t r, int v, int s) { __builtin_amdgcn_raw_buffer_store_b128(x, r, v, s, AUX); }
template <int AUX> DEV void st8(v2u x, rsrc_t r, int v, int s) { __builtin_amdgcn_raw_buffer_store_b64(x, r, v, s, AUX); }
DEV v4u lds16(LAS char* p) { return *(LAS v4u*)p; }
DEV f4 ldsf4(LAS char* p) { return *(LAS f4*)p; }
DEV void sts16(LAS char* p, v4u x) { *(LAS v4u*)p = x; }
DEV v4u trfrag(LAS char* p, int rs4) {
  s4 a = __builtin_amdgcn_ds_read_tr16_b64_v4i16((LAS s4*)p);
  s4 b = __builtin_amdgcn_ds_read_tr16_b64_v4i16((LAS s4*)(p + rs4));
  v2u x = __builtin_bit_cast(v2u, a), y = __builtin_bit_cast(v2u, b);
  return v4u{x.x, x.y, y.x, y.y};
}
DEV int fsw(int r) { return (r & 2) | ((r >> 1) & 4); }
#define FENCE() __builtin_amdgcn_sched_barrier(0)
DEV void bar() {
  asm volatile("s_waitcnt lgkmcnt(0)" ::: "memory");
  __builtin_amdgcn_s_barrier();
  asm volatile("" ::: "memory");
  FENCE();
}
DEV void drain() { asm volatile("s_waitcnt vmcnt(0)" ::: "memory"); }
DEV u64 aload(const char* ws, int off) {
  return __hip_atomic_load((GAS u64*)(ws + off), __ATOMIC_RELAXED, __HIP_MEMORY_SCOPE_AGENT);
}
DEV void astore(char* ws, int off, u64 v) {
  __hip_atomic_store((GAS u64*)(ws + off), v, __ATOMIC_RELAXED, __HIP_MEMORY_SCOPE_AGENT);
}
DEV void poll(const char* ws, int off, bool act, u64 tag) {
  for (u32 s = 0; s < SPIN_CAP; ++s) {
    u64 v = tag;
    if (act) v = aload(ws, off);
    if (__all(v == tag)) break;
  }
  __builtin_amdgcn_fence(__ATOMIC_ACQUIRE, "wavefront");
}
template <int CTRL> DEV float dpp(float v) {
  return __builtin_bit_cast(float, __builtin_amdgcn_update_dpp(0, __builtin_bit_cast(int, v), CTRL, 0xf, 0xf, false));
}
DEV float sum16(float v) {
  v += dpp<0x128>(v); v += dpp<0x124>(v); v += dpp<0x122>(v); v += dpp<0x121>(v);
  return v;
}
DEV float xg_sum(float v) {
  u32 u = asu(v);
  auto a = __builtin_amdgcn_permlane16_swap(u, u, false, false);
  v = asf(a[0]) + asf(a[1]);
  u = asu(v);
  auto b = __builtin_amdgcn_permlane32_swap(u, u, false, false);
  return asf(b[0]) + asf(b[1]);
}
DEV float xg_max(float v) {
  u32 u = asu(v);
  auto a = __builtin_amdgcn_permlane16_swap(u, u, false, false);
  v = fmaxf(asf(a[0]), asf(a[1]));
  u = asu(v);
  auto b = __builtin_amdgcn_permlane32_swap(u, u, false, false);
  return fmaxf(asf(b[0]), asf(b[1]));
}
DEV v4u pair_swap(v2u p0, v2u p1) {
  auto a = __builtin_amdgcn_permlane16_swap(p0.x, p1.x, false, false);
  auto b = __builtin_amdgcn_permlane16_swap(p0.y, p1.y, false, false);
  return v4u{a[0], b[0], a[1], b[1]};
}
DEV v2u pk4(f4 a) { return v2u{pk(a[0], a[1]), pk(a[2], a[3])}; }

template <bool ONE_B>
DEV void gather_mod(const char* ws, LAS char* dst, int b, int ca, int cb, u64 tag, int tid) {
  u64 v[3];
  int off[3];
#pragma unroll
  for (int i = 0; i < 3; ++i) {
    int n = tid + 256 * i;
    int col = n < 384 ? ca * 384 + n : cb * 384 + n - 384;
    off[i] = WS_MOD + (b * 2304 + col) * 8;
  }
  const u64 want = tag << 16;
  for (u32 s = 0; s < SPIN_CAP; ++s) {
#pragma unroll
    for (int i = 0; i < 3; ++i) v[i] = aload(ws, off[i]);
    bool ok = ((v[0] ^ want) >> 16) == 0 && ((v[1] ^ want) >> 16) == 0 && ((v[2] ^ want) >> 16) == 0;
    if (__all(ok)) break;
  }
#pragma unroll
  for (int i = 0; i < 3; ++i) {
    int n = tid + 256 * i;
    float f = lo((u32)v[i]);
    if (ONE_B && n >= 384) f = rbf(1.f + f);
    *(LAS float*)(dst + n * 4) = f;
  }
}

#define TS(i) do {} while (0)

template <bool VTYPE>
DEV void phaseA_gemm(const v4u (&wf)[12][3], LAS char* L, int rot, int lane, int w, rsrc_t rw, int dst, float scale) {
  const int i16 = lane & 15, g = lane >> 4;
  const int voff = VTYPE ? lane * 16 : (g >> 1) * 1024 + ((g & 1) * 16 + i16) * 16;
#pragma unroll
  for (int tp = 0; tp < 2; ++tp) {
    f4 acc[2][3];
#pragma unroll
    for (int t = 0; t < 2; ++t)
#pragma unroll
      for (int ft = 0; ft < 3; ++ft) acc[t][ft] = f4{0.f, 0.f, 0.f, 0.f};
#pragma unroll
    for (int s = 0; s < 12; ++s) {
      int ks = s + rot; if (ks >= 12) ks -= 12;
      const int kc = 4 * ks + g;
#pragma unroll
      for (int t = 0; t < 2; ++t) {
        v4u hb = lds16(L + L_ACT + kc * 1024 + (32 * tp + 16 * t) * 16 + ((i16 ^ (kc & 7)) * 16));
#pragma unroll
        for (int ft = 0; ft < 3; ++ft) acc[t][ft] = VTYPE ? mfma(hb, wf[s][ft], acc[t][ft]) : mfma(wf[s][ft], hb, acc[t][ft]);
      }
    }
#pragma unroll
    for (int ft = 0; ft < 3; ++ft) {
      if (VTYPE) {
        v2u a = pk4(acc[0][ft]), b = pk4(acc[1][ft]);
        st16<SC1>(v4u{a.x, a.y, b.x, b.y}, rw, voff, dst + (tp * 12 + 3 * w + ft) * 1024);
      } else {
        v4u o = pair_swap(pk4(acc[0][ft] * scale), pk4(acc[1][ft] * scale));
        st16<SC1>(o, rw, voff, dst + (2 * (3 * w + ft)) * 1024 + tp * 512);
      }
    }
  }
}

__global__ void __launch_bounds__(256, 1)
dit_kernel(const u16* __restrict__ x, const u16* __restrict__ cvec, const u16* __restrict__ wqkv, const u16* __restrict__ wo,
           const u16* __restrict__ wmod, const u16* __restrict__ w1, const u16* __restrict__ w2, u16* __restrict__ out, char* ws) {
  extern __shared__ __attribute__((aligned(16))) char smem[];
  LAS char* L = (LAS char*)smem;
  const int tid = threadIdx.x, lane = tid & 63, w = __builtin_amdgcn_readfirstlane(tid >> 6);
  const int i16 = lane & 15, g = lane >> 4;
  const int xcd = blockIdx.x & 7, idx = blockIdx.x >> 3;
  const int team = xcd * 4 + (idx >> 3), mem = idx & 7, bat = team >> 4, me = team * 8 + mem;
  const rsrc_t rw = mkrs(ws, WS_END);
  const rsrc_t rx = mkrs(x, 2048 * 384 * 2);
  TS(0);
  const u64 e0 = aload(ws, WS_EPOCH);
  u64 touch = 0;
  if (tid < 4) touch = aload(ws, WS_FLAG + me * 512 + tid * 128);
  u64 tag;

  if (mem < 6) {
    const rsrc_t rq = mkrs(wqkv, 384 * 1152 * 2);
    v4u xr[4][3];
#pragma unroll
    for (int it = 0; it < 4; ++it)
#pragma unroll
      for (int i = 0; i < 3; ++i) xr[it][i] = ld16<0>(rx, (g * 384 + i16 * 8) * 2 + i * 256, (team * 64 + w * 16 + it * 4) * 768);
    int woff[3], loff[3];
#pragma unroll
    for (int i = 0; i < 3; ++i) {
      int q = i * 256 + tid, row = q / 24, c = q % 24;
      woff[i] = row * 2304 + c * 16;
      loff[i] = row * 384 + (c ^ fsw(row)) * 16;
    }
    const int rot = team % 12;
    v4u wst[12][3];
    auto issue = [&](int r) {
#pragma unroll
      for (int s = 3 * r; s < 3 * r + 3; ++s) {
        int ks = s + rot; if (ks >= 12) ks -= 12;
#pragma unroll
        for (int i = 0; i < 3; ++i) wst[s][i] = ld16<0>(rq, woff[i], ks * 32 * 2304 + mem * 384);
      }
      FENCE();
    };
    float xn[4][24];
    auto lnorm = [&](int it) {
      float s = 0.f;
#pragma unroll
      for (int i = 0; i < 3; ++i)
#pragma unroll
        for (int e = 0; e < 4; ++e) {
          xn[it][i * 8 + 2 * e] = lo(xr[it][i][e]);
          xn[it][i * 8 + 2 * e + 1] = hi(xr[it][i][e]);
          s += xn[it][i * 8 + 2 * e] + xn[it][i * 8 + 2 * e + 1];
        }
      const float mu = sum16(s) * (1.f / 384.f);
      float q = 0.f;
#pragma unroll
      for (int e = 0; e < 24; ++e) { xn[it][e] -= mu; q += xn[it][e] * xn[it][e]; }
      const float r = rsqrtf(sum16(q) * (1.f / 384.f) + 1e-5f);
#pragma unroll
      for (int e = 0; e < 24; ++e) xn[it][e] *= r;
      FENCE();
    };
    v4u wf[12][3];
    int tra[3];
#pragma unroll
    for (int ft = 0; ft < 3; ++ft) {
      int row = 8 * g + (i16 >> 2), ch = 2 * (3 * w + ft) + ((lane & 3) >> 1);
      tra[ft] = row * 384 + (ch ^ fsw(row)) * 16 + 8 * (lane & 1);
    }
    auto stage = [&](int r) {
      LAS char* B = L + L_WB + (r & 1) * 36864;
#pragma unroll
      for (int s = 0; s < 3; ++s)
#pragma unroll
        for (int i = 0; i < 3; ++i) sts16(B + s * 12288 + loff[i], wst[3 * r + s][i]);
      bar();
#pragma unroll
      for (int s = 0; s < 3; ++s)
#pragma unroll
        for (int ft = 0; ft < 3; ++ft) wf[3 * r + s][ft] = trfrag(B + s * 12288 + tra[ft], 4 * 384);
      FENCE();
    };
    FENCE();
    issue(0);
    tag = __builtin_amdgcn_readfirstlane((u32)e0) | ((u64)__builtin_amdgcn_readfirstlane((u32)(e0 >> 32)) << 32);
    tag += 1;
    if (tid == 0) astore(ws, WS_START + blockIdx.x * 8, tag);
    lnorm(0); lnorm(1);
    issue(1);
    lnorm(2); lnorm(3);
    issue(2);
    stage(0);
    issue(3);
    stage(1); stage(2); stage(3);
    TS(1);
    gather_mod<true>(ws, L + L_MODA, bat, 0, 1, tag, tid);
    bar();
    TS(2);
#pragma unroll
    for (int i = 0; i < 3; ++i) {
      const int c = i16 + 16 * i;
      f4 t0 = ldsf4(L + L_MODA + c * 32), t1 = ldsf4(L + L_MODA + c * 32 + 16);
      f4 g0 = ldsf4(L + L_MODA + 1536 + c * 32), g1 = ldsf4(L + L_MODA + 1536 + c * 32 + 16);
#pragma unroll
      for (int it = 0; it < 4; ++it) {
        const float* v = &xn[it][i * 8];
        v4u h = {pk(v[0] * g0[0] + t0[0], v[1] * g0[1] + t0[1]), pk(v[2] * g0[2] + t0[2], v[3] * g0[3] + t0[3]),
                 pk(v[4] * g1[0] + t1[0], v[5] * g1[1] + t1[1]), pk(v[6] * g1[2] + t1[2], v[7] * g1[3] + t1[3])};
        sts16(L + L_ACT + c * 1024 + (((w * 16 + it * 4 + g) ^ (c & 7)) * 16), h);
      }
    }
    bar();
    TS(3);
    const int hd = mem & 1;
    if (mem >= 4) phaseA_gemm<true>(wf, L, rot, lane, w, rw, WS_V + (team * 2 + hd) * 24576, 1.f);
    else phaseA_gemm<false>(wf, L, rot, lane, w, rw, (mem < 2 ? WS_Q : WS_K) + (team * 2 + hd) * 24576,
                            mem < 2 ? 0.07216878364870322f * 1.4426950408889634f : 1.f);
    TS(4);
    drain();
    bar();
    if (w == 0) {
      if (mem < 2) {
        if (lane < 4) astore(ws, WS_FLAG + (team * 8 + hd * 4 + lane) * 512, tag);
      } else if (lane < 16) {
        const int cons = (bat * 16 + lane) * 8 + hd * 4 + ((team >> 2) & 3);
        astore(ws, WS_FLAG + cons * 512 + ((mem < 4 ? 1 : 5) + (team & 3)) * 8, tag);
      }
    }
    TS(5);
  } else {
    const int hidx = team * 2 + (mem - 6);
    const int srow = tid >> 3, sc = tid & 7;
    const int sl = srow * 128 + (sc ^ fsw(srow)) * 16;
    const int trr = 8 * g + (i16 >> 2);
    const int tra = trr * 128 + ((2 * w + ((lane & 3) >> 1)) ^ fsw(trr)) * 16 + 8 * (lane & 1);
    v4u st[12];
    u16 cv[3];
    if (hidx < 36) {
      const rsrc_t rm = mkrs(wmod, 384 * 2304 * 2);
#pragma unroll
      for (int i = 0; i < 3; ++i) cv[i] = cvec[tid + 256 * i];
#pragma unroll
      for (int s = 0; s < 12; ++s) st[s] = ld16<0>(rm, srow * 4608 + sc * 16, s * 32 * 4608 + hidx * 128);
      FENCE();
    }
    tag = __builtin_amdgcn_readfirstlane((u32)e0) | ((u64)__builtin_amdgcn_readfirstlane((u32)(e0 >> 32)) << 32);
    tag += 1;
    if (tid == 0) astore(ws, WS_START + blockIdx.x * 8, tag);
    if (hidx < 36) {
#pragma unroll
      for (int i = 0; i < 3; ++i) {
        float c = lo(cv[i]);
        *(LAS u16*)(L + L_SC + (tid + 256 * i) * 2) = (u16)pk(c / (1.f + __expf(-c)), 0.f);
      }
#pragma unroll
      for (int s = 0; s < 12; ++s) sts16(L + L_WB + s * 4096 + sl, st[s]);
      bar();
      f4 acc = {0.f, 0.f, 0.f, 0.f};
#pragma unroll
      for (int s = 0; s < 12; ++s)
        acc = mfma(lds16(L + L_SC + (i16 & 1) * 768 + s * 64 + g * 16), trfrag(L + L_WB + s * 4096 + tra, 4 * 128), acc);
      if (lane < 16) {
        const u32 p = pk(acc[0], acc[1]);
        const int col = hidx * 64 + w * 16 + lane;
        astore(ws, WS_MOD + col * 8, (tag << 16) | (p & 0xffffu));
        astore(ws, WS_MOD + (2304 + col) * 8, (tag << 16) | (p >> 16));
      }
      FENCE();
    }
    TS(1);
    const int r = 63 - hidx;
    if (r < 54) {
      const u16* src; int stride, soff;
      if (r < 6) { src = wo; stride = 768; soff = r * 128; }
      else if (r < 30) { src = w1; stride = 3072; soff = (r - 6) * 128; }
      else { src = w2; stride = 768; soff = ((r - 30) / 6) * 384 * 768 + ((r - 30) % 6) * 128; }
      const rsrc_t rs = mkrs(src, r < 6 ? 384 * 384 * 2 : 384 * 1536 * 2);
#pragma unroll
      for (int s = 0; s < 12; ++s) st[s] = ld16<0>(rs, srow * stride + sc * 16, s * 32 * stride + soff);
      FENCE();
      const bool isw1 = r >= 6 && r < 30;
      if (isw1) {
        gather_mod<true>(ws, L + L_MODA, 0, 3, 4, tag, tid);
        gather_mod<true>(ws, L + L_MODB, 1, 3, 4, tag, tid);
      }
#pragma unroll
      for (int s = 0; s < 12; ++s) sts16(L + L_WB + 49152 + s * 4096 + sl, st[s]);
      bar();
      v4u f[12];
#pragma unroll
      for (int s = 0; s < 12; ++s) f[s] = trfrag(L + L_WB + 49152 + s * 4096 + tra, 4 * 128);
      if (!isw1) {
        int dst, kstride;
        if (r < 6) { dst = WS_WOT + (4 * r + w) * 12288; kstride = 1024; }
        else {
          const int ft = 4 * ((r - 30) % 6) + w;
          dst = WS_W2T + (((ft / 6) * 4 + (r - 30) / 6) * 72 + ft % 6) * 1024; kstride = 6144;
        }
#pragma unroll
        for (int s = 0; s < 12; ++s) st16<SC1>(f[s], rw, lane * 16, dst + s * kstride);
      } else {
        const int ft = 4 * (r - 6) + w;
        const v4u ones = {0x3f803f80u, 0x3f803f80u, 0x3f803f80u, 0x3f803f80u};
        f4 av = {0.f, 0.f, 0.f, 0.f}, au[2] = {{0.f, 0.f, 0.f, 0.f}, {0.f, 0.f, 0.f, 0.f}};
#pragma unroll
        for (int s = 0; s < 12; ++s) {
          LAS char* tb = L + ((i16 & 1) ? L_MODB : L_MODA) + s * 128 + g * 32;
          f4 t0 = ldsf4(tb), t1 = ldsf4(tb + 16);
          av = mfma(f[s], v4u{pk(t0[0], t0[1]), pk(t0[2], t0[3]), pk(t1[0], t1[1]), pk(t1[2], t1[3])}, av);
#pragma unroll
          for (int b = 0; b < 2; ++b) {
            LAS char* gb = L + (b ? L_MODB : L_MODA) + 1536 + s * 128 + g * 32;
            f4 g0 = ldsf4(gb), g1 = ldsf4(gb + 16);
            v4u sc4 = {pk(lo(f[s].x) * g0[0], hi(f[s].x) * g0[1]), pk(lo(f[s].y) * g0[2], hi(f[s].y) * g0[3]),
                       pk(lo(f[s].z) * g1[0], hi(f[s].z) * g1[1]), pk(lo(f[s].w) * g1[2], hi(f[s].w) * g1[3])};
            au[b] = mfma(sc4, ones, au[b]);
            st16<SC1>(sc4, rw, lane * 16, WS_W1T + ((b * 96 + ft) * 12 + s) * 1024);
          }
        }
        if (i16 < 2) {
          const f4 u = i16 ? au[1] : au[0];
          st16<SC1>(__builtin_bit_cast(v4u, u), rw, i16 * 96 * 128 + g * 16, WS_UV + ft * 128);
          st16<SC1>(__builtin_bit_cast(v4u, av), rw, i16 * 96 * 128 + g * 16 + 64, WS_UV + ft * 128);
        }
      }
      drain();
      bar();
      if (tid == 0) astore(ws, WS_RTF + r * 8, tag);
    }
    if (hidx == 0 && w == 0) {
#pragma unroll
      for (int i = 0; i < 4; ++i) poll(ws, WS_START + (lane + 64 * i) * 8, true, tag);
      if (lane == 0) astore(ws, WS_EPOCH, tag);
    }
    TS(5);
  }
  asm volatile("" :: "v"(touch));

  const int c_tt = mem >> 1, c_ch = mem & 1;
  gather_mod<false>(ws, L + L_GATE, bat, 2, 5, tag, tid);
  v2u xres[3];
#pragma unroll
  for (int ft = 0; ft < 3; ++ft)
    xres[ft] = ld8<0>(rx, i16 * 768 + g * 8 + ft * 32, (team * 64 + c_tt * 16) * 768 + (c_ch * 192 + w * 48) * 2);
  if (w == 0) poll(ws, WS_RTF + lane * 8, lane < 6, tag);
  bar();
  v4u wof[3][12];
#pragma unroll
  for (int ft = 0; ft < 3; ++ft)
#pragma unroll
    for (int s = 0; s < 12; ++s) wof[ft][s] = ld16<SC1>(rw, lane * 16, WS_WOT + ((c_ch * 12 + w * 3 + ft) * 12 + s) * 1024);
  FENCE();
  TS(6);
  if (w == 0) poll(ws, WS_FLAG + me * 512 + lane * 8, lane < 9, tag);
  bar();
  TS(7);

  {
    const int hd = mem >> 2, sp = mem & 3;
    const int kt0 = bat * 16 + sp * 4;
    v4u qs[6];
#pragma unroll
    for (int i = 0; i < 6; ++i) qs[i] = ld16<SC1>(rw, tid * 16, WS_Q + (team * 2 + hd) * 24576 + i * 4096);
    v4u kf[6][4];
    const int fo = g * 1024 + i16 * 16;
    const int kbase = WS_K + ((kt0 + w) * 2 + hd) * 24576;
    auto kload = [&](int ds) {
#pragma unroll
      for (int kt = 0; kt < 4; ++kt) kf[ds][kt] = ld16<SC1>(rw, fo + kt * 256, kbase + ds * 4096);
    };
    v4u vf[8][3];
    auto vload = [&](int ks) {
#pragma unroll
      for (int dt = 0; dt < 3; ++dt)
        vf[ks][dt] = ld16<SC1>(rw, lane * 16 + dt * 1024, WS_V + ((kt0 + (ks >> 1)) * 2 + hd) * 24576 + ((ks & 1) * 12 + 3 * w) * 1024);
    };
    kload(0); kload(1);
    FENCE();
#pragma unroll
    for (int i = 0; i < 6; ++i) sts16(L + L_Q + i * 4096 + tid * 16, qs[i]);
    bar();
    f4 sa[4][4];
#pragma unroll
    for (int kt = 0; kt < 4; ++kt)
#pragma unroll
      for (int qt = 0; qt < 4; ++qt) sa[kt][qt] = f4{0.f, 0.f, 0.f, 0.f};
#pragma unroll
    for (int ds = 0; ds < 6; ++ds) {
      if (ds < 4) kload(ds + 2); else vload(ds - 4);
      FENCE();
#pragma unroll
      for (int qt = 0; qt < 4; ++qt) {
        v4u qb = lds16(L + L_Q + ds * 4096 + fo + qt * 256);
#pragma unroll
        for (int kt = 0; kt < 4; ++kt) sa[kt][qt] = mfma(kf[ds][kt], qb, sa[kt][qt]);
      }
      FENCE();
    }
    TS(8);
    vload(2); vload(3);
    FENCE();
#pragma unroll
    for (int qt = 0; qt < 4; ++qt) {
      float m = fmaxf(fmaxf(sa[0][qt][0], sa[0][qt][1]), fmaxf(sa[0][qt][2], sa[0][qt][3]));
#pragma unroll
      for (int kt = 1; kt < 4; ++kt)
        m = fmaxf(m, fmaxf(fmaxf(sa[kt][qt][0], sa[kt][qt][1]), fmaxf(sa[kt][qt][2], sa[kt][qt][3])));
      m = xg_max(m);
      if (g == 0) *(LAS float*)(L + L_MX + (qt * 16 + i16) * 16 + w * 4) = m;
    }
    bar();
    float mw = 0.f;
#pragma unroll
    for (int qt = 0; qt < 4; ++qt) {
      vload(4 + qt);
      FENCE();
      const f4 m4 = ldsf4(L + L_MX + (qt * 16 + i16) * 16);
      const float m = fmaxf(fmaxf(m4[0], m4[1]), fmaxf(m4[2], m4[3]));
      if (qt == w) mw = m;
#pragma unroll
      for (int u = 0; u < 2; ++u) {
        float p[8];
#pragma unroll
        for (int e = 0; e < 4; ++e) {
          p[e] = __builtin_amdgcn_exp2f(sa[2 * u][qt][e] - m);
          p[4 + e] = __builtin_amdgcn_exp2f(sa[2 * u + 1][qt][e] - m);
        }
        sts16(L + L_P + ((2 * w + u) * 4 + qt) * 1024 + lane * 16, v4u{pk(p[0], p[1]), pk(p[2], p[3]), pk(p[4], p[5]), pk(p[6], p[7])});
      }
      FENCE();
    }
    bar();
    TS(9);
    const int odst = WS_OP + ((team * 2 + hd) * 4 + sp) * 24576;
    const int voff = (g >> 1) * 1024 + ((g & 1) * 16 + i16) * 16;
    const v4u ones = {0x3f803f80u, 0x3f803f80u, 0x3f803f80u, 0x3f803f80u};
    f4 lsum = {0.f, 0.f, 0.f, 0.f};
#pragma unroll
    for (int tp = 0; tp < 2; ++tp) {
      f4 oa[2][3];
#pragma unroll
      for (int t = 0; t < 2; ++t)
#pragma unroll
        for (int dt = 0; dt < 3; ++dt) oa[t][dt] = f4{0.f, 0.f, 0.f, 0.f};
#pragma unroll
      for (int ks = 0; ks < 8; ++ks) {
#pragma unroll
        for (int t = 0; t < 2; ++t) {
          v4u pb = lds16(L + L_P + (ks * 4 + 2 * tp + t) * 1024 + lane * 16);
#pragma unroll
          for (int dt = 0; dt < 3; ++dt) oa[t][dt] = mfma(vf[ks][dt], pb, oa[t][dt]);
        }
        if (tp == 0) lsum = mfma(ones, lds16(L + L_P + (ks * 4 + w) * 1024 + lane * 16), lsum);
      }
#pragma unroll
      for (int dt = 0; dt < 3; ++dt)
        st16<SC1>(pair_swap(pk4(oa[0][dt]), pk4(oa[1][dt])), rw, voff, odst + 2 * (3 * w + dt) * 1024 + tp * 512);
      if (tp == 0 && g == 0)
        st8<SC1>(v2u{asu(mw), asu(lsum[0])}, rw, i16 * 8,
                 WS_ML + ((team * 2 + hd) * 4 + sp) * 512 + w * 128);
    }
    TS(10);
    drain();
    bar();
    if (tid < 8) astore(ws, WS_FLAG + (team * 8 + tid) * 512 + 128 + mem * 8, tag);
    TS(11);
  }

  if (w == 0) poll(ws, WS_RTF + lane * 8, lane >= 6 && lane < 54, tag);
  bar();
  v4u w1f[3][12];
  f4 uu[3], vv[3];
#pragma unroll
  for (int ft = 0; ft < 3; ++ft) {
    const int gft = bat * 96 + mem * 12 + w * 3 + ft;
    uu[ft] = __builtin_bit_cast(f4, ld16<SC1>(rw, g * 16, WS_UV + gft * 128));
    vv[ft] = __builtin_bit_cast(f4, ld16<SC1>(rw, g * 16 + 64, WS_UV + gft * 128));
#pragma unroll
    for (int s = 0; s < 12; ++s) w1f[ft][s] = ld16<SC1>(rw, lane * 16, WS_W1T + (gft * 12 + s) * 1024);
  }
  FENCE();
  TS(12);
  if (w == 0) poll(ws, WS_FLAG + me * 512 + 128 + lane * 8, lane < 8, tag);
  bar();
  TS(13);

  {
    const int hd = w >> 1;
    const int obase = WS_OP + (team * 2 + hd) * 4 * 24576 + (w & 1) * 12288 + c_tt * 256;
    v2u ml[4];
#pragma unroll
    for (int s = 0; s < 4; ++s) ml[s] = ld8<SC1>(rw, i16 * 8, WS_ML + ((team * 2 + hd) * 4 + s) * 512 + c_tt * 128);
    v4u op[3][4];
#pragma unroll
    for (int i = 0; i < 3; ++i)
#pragma unroll
      for (int s = 0; s < 4; ++s) op[i][s] = ld16<SC1>(rw, g * 1024 + i16 * 16, obase + s * 24576 + i * 4096);
    FENCE();
    float wt[4];
    {
      float m = -3.0e38f;
#pragma unroll
      for (int s = 0; s < 4; ++s) m = fmaxf(m, asf(ml[s].x));
      float den = 0.f;
#pragma unroll
      for (int s = 0; s < 4; ++s) {
        wt[s] = __builtin_amdgcn_exp2f(asf(ml[s].x) - m);
        den += wt[s] * asf(ml[s].y);
      }
      const float inv = 1.f / den;
#pragma unroll
      for (int s = 0; s < 4; ++s) wt[s] *= inv;
    }
#pragma unroll
    for (int i = 0; i < 3; ++i) {
      v4u o;
#pragma unroll
      for (int e = 0; e < 4; ++e) {
        float a = lo(op[i][0][e]) * wt[0], b = hi(op[i][0][e]) * wt[0];
#pragma unroll
        for (int s = 1; s < 4; ++s) { a += lo(op[i][s][e]) * wt[s]; b += hi(op[i][s][e]) * wt[s]; }
        o[e] = pk(a, b);
      }
      sts16(L + L_OC + (3 * w + i) * 1024 + lane * 16, o);
    }
    bar();
    f4 acc[3] = {{0.f, 0.f, 0.f, 0.f}, {0.f, 0.f, 0.f, 0.f}, {0.f, 0.f, 0.f, 0.f}};
#pragma unroll
    for (int s = 0; s < 12; ++s) {
      v4u ob = lds16(L + L_OC + s * 1024 + lane * 16);
#pragma unroll
      for (int ft = 0; ft < 3; ++ft) acc[ft] = mfma(wof[ft][s], ob, acc[ft]);
    }
    v2u xp[3];
    float s1 = 0.f, s2 = 0.f;
#pragma unroll
    for (int ft = 0; ft < 3; ++ft) {
      const f4 ga = ldsf4(L + L_GATE + (c_ch * 192 + w * 48 + ft * 16 + g * 4) * 4);
      xp[ft] = v2u{pk(lo(xres[ft].x) + ga[0] * acc[ft][0], hi(xres[ft].x) + ga[1] * acc[ft][1]),
                   pk(lo(xres[ft].y) + ga[2] * acc[ft][2], hi(xres[ft].y) + ga[3] * acc[ft][3])};
      const float a = lo(xp[ft].x), b = hi(xp[ft].x), c = lo(xp[ft].y), d = hi(xp[ft].y);
      s1 += (a + b) + (c + d);
      s2 += (a * a + b * b) + (c * c + d * d);
    }
    const int xdst = WS_X1 + team * 49152 + (c_ch * 24 + w * 6) * 1024 + c_tt * 256;
    st16<SC1>(pair_swap(xp[0], xp[1]), rw, ((g & 1) * 2 + (g >> 1)) * 1024 + i16 * 16, xdst);
    const v4u third = pair_swap(xp[2], xp[2]);
    if ((g & 1) == 0) st16<SC1>(third, rw, (4 + (g >> 1)) * 1024 + i16 * 16, xdst);
    s1 = xg_sum(s1); s2 = xg_sum(s2);
    if (g == 0)
      st8<SC1>(v2u{asu(s1), asu(s2)}, rw, i16 * 8,
               WS_ST + team * 4096 + (c_tt * 8 + c_ch * 4 + w) * 128);
    TS(14);
    drain();
    bar();
    if (tid < 8) astore(ws, WS_FLAG + (team * 8 + tid) * 512 + 256 + mem * 8, tag);
    TS(15);
  }

  const int e_th = mem & 1, e_cs = mem >> 1;
  v4u w2f[12][6];
  const int w2base = WS_W2T + (e_cs * 4 + w) * 72 * 1024;
#pragma unroll
  for (int s = 0; s < 6; ++s)
#pragma unroll
    for (int ft = 0; ft < 6; ++ft) w2f[s][ft] = ld16<SC1>(rw, lane * 16, w2base + (s * 6 + ft) * 1024);
  FENCE();
  TS(16);
  if (w == 0) poll(ws, WS_FLAG + me * 512 + 256 + lane * 8, lane < 8, tag);
  bar();
  TS(17);

  v4u x1r[2];
  {
    const v4u stv = ld16<SC1>(rw, tid * 16, WS_ST + team * 4096);
    v4u xs[12];
#pragma unroll
    for (int i = 0; i < 12; ++i) xs[i] = ld16<SC1>(rw, tid * 16, WS_X1 + team * 49152 + i * 4096);
    FENCE();
    {
      float a[4];
#pragma unroll
      for (int e = 0; e < 4; ++e) { a[e] = asf(stv[e]); a[e] += dpp<0x128>(a[e]); a[e] = xg_sum(a[e]); }
      const float mu0 = a[0] * (1.f / 384.f), mu1 = a[2] * (1.f / 384.f);
      const float r0 = rsqrtf(fmaxf(a[1] * (1.f / 384.f) - mu0 * mu0, 0.f) + 1e-5f);
      const float r1 = rsqrtf(fmaxf(a[3] * (1.f / 384.f) - mu1 * mu1, 0.f) + 1e-5f);
      if (lane < 8) *(LAS f4*)(L + L_STL + (w * 16 + lane * 2) * 8) = f4{r0, -r0 * mu0, r1, -r1 * mu1};
    }
#pragma unroll
    for (int i = 0; i < 12; ++i) sts16(L + L_ACT + i * 4096 + tid * 16, xs[i]);
    bar();
    TS(18);
#pragma unroll
    for (int i = 0; i < 2; ++i) {
      int n = tid + 256 * i; if (n >= 384) n = 383;
      x1r[i] = lds16(L + L_ACT + (e_cs * 12 + n % 12) * 1024 + (e_th * 32 + n / 12) * 16);
    }
    const int hdst = WS_HH + team * 196608 + (mem * 24 + w * 6) * 1024;
    const int voff = (g >> 1) * 1024 + ((g & 1) * 16 + i16) * 16;
#pragma unroll
    for (int tp = 0; tp < 2; ++tp) {
      v2u hp[2][3];
#pragma unroll
      for (int t = 0; t < 2; ++t) {
        f4 acc[3] = {{0.f, 0.f, 0.f, 0.f}, {0.f, 0.f, 0.f, 0.f}, {0.f, 0.f, 0.f, 0.f}};
#pragma unroll
        for (int s = 0; s < 12; ++s) {
          v4u xb = lds16(L + L_ACT + (4 * s + g) * 1024 + ((2 * tp + t) * 16 + i16) * 16);
#pragma unroll
          for (int ft = 0; ft < 3; ++ft) acc[ft] = mfma(w1f[ft][s], xb, acc[ft]);
        }
        const f2 rb = *(LAS f2*)(L + L_STL + ((2 * tp + t) * 16 + i16) * 8);
#pragma unroll
        for (int ft = 0; ft < 3; ++ft) {
          f4 y;
#pragma unroll
          for (int e = 0; e < 4; ++e) {
            const float z = rb[0] * acc[ft][e] + (rb[1] * uu[ft][e] + vv[ft][e]);
            const float a = z * (z * z * (-0.10294324f) + (-2.30220819f));
            y[e] = z * __builtin_amdgcn_rcpf(1.f + __builtin_amdgcn_exp2f(a));
          }
          hp[t][ft] = pk4(y);
        }
      }
#pragma unroll
      for (int ft = 0; ft < 3; ++ft) st16<SC1>(pair_swap(hp[0][ft], hp[1][ft]), rw, voff, hdst + 2 * ft * 1024 + tp * 512);
    }
    TS(19);
    drain();
    bar();
    if (tid < 8) astore(ws, WS_FLAG + (team * 8 + tid) * 512 + 384 + mem * 8, tag);
    TS(20);
  }

#pragma unroll
  for (int s = 6; s < 12; ++s)
#pragma unroll
    for (int ft = 0; ft < 6; ++ft) w2f[s][ft] = ld16<SC1>(rw, lane * 16, w2base + (s * 6 + ft) * 1024);
  FENCE();
  TS(21);
  if (w == 0) poll(ws, WS_FLAG + me * 512 + 384 + lane * 8, lane < 8, tag);
  bar();
  TS(22);

  {
    v4u hb[2][12];
    const int hsrc = WS_HH + team * 196608 + w * 48 * 1024 + e_th * 512;
#pragma unroll
    for (int t = 0; t < 2; ++t)
#pragma unroll
      for (int s = 0; s < 12; ++s) hb[t][s] = ld16<SC1>(rw, g * 1024 + i16 * 16 + t * 256, hsrc + s * 4096);
    FENCE();
#pragma unroll
    for (int t = 0; t < 2; ++t) {
      f4 acc[6];
#pragma unroll
      for (int ft = 0; ft < 6; ++ft) acc[ft] = f4{0.f, 0.f, 0.f, 0.f};
#pragma unroll
      for (int s = 0; s < 12; ++s)
#pragma unroll
        for (int ft = 0; ft < 6; ++ft) acc[ft] = mfma(w2f[s][ft], hb[t][s], acc[ft]);
#pragma unroll
      for (int ft = 0; ft < 6; ++ft) *(LAS f4*)(L + L_RED + w * 12800 + (t * 16 + i16) * 400 + ft * 64 + g * 16) = acc[ft];
    }
    bar();
    TS(23);
    const rsrc_t ro = mkrs(out, 2048 * 384 * 2);
#pragma unroll
    for (int i = 0; i < 2; ++i) {
      const int n = tid + 256 * i;
      if (n < 384) {
        const int tok = n / 12, c8 = n % 12;
        f4 a = {0.f, 0.f, 0.f, 0.f}, b = {0.f, 0.f, 0.f, 0.f};
#pragma unroll
        for (int q = 0; q < 4; ++q) {
          a += ldsf4(L + L_RED + q * 12800 + tok * 400 + c8 * 32);
          b += ldsf4(L + L_RED + q * 12800 + tok * 400 + c8 * 32 + 16);
        }
        const f4 g0 = ldsf4(L + L_GATE + 1536 + (e_cs * 96 + c8 * 8) * 4), g1 = ldsf4(L + L_GATE + 1536 + (e_cs * 96 + c8 * 8) * 4 + 16);
        const v4u xr = x1r[i];
        v4u o = {pk(lo(xr.x) + g0[0] * a[0], hi(xr.x) + g0[1] * a[1]), pk(lo(xr.y) + g0[2] * a[2], hi(xr.y) + g0[3] * a[3]),
                 pk(lo(xr.z) + g1[0] * b[0], hi(xr.z) + g1[1] * b[1]), pk(lo(xr.w) + g1[2] * b[2], hi(xr.w) + g1[3] * b[3])};
        st16<0>(o, ro, tok * 768 + c8 * 16, (team * 64 + e_th * 32) * 768 + e_cs * 192);
      }
    }
    TS(24);
  }
}

}

extern "C" void kernel_launch(void* const* d_in, const int* in_sizes, int n_in, void* d_out, int out_size, void* d_ws,
                              size_t ws_size, hipStream_t stream) {
  (void)in_sizes; (void)n_in; (void)out_size; (void)ws_size;
  (void)hipFuncSetAttribute((const void*)dit::dit_kernel, hipFuncAttributeMaxDynamicSharedMemorySize, dit::L_BYTES);
  dit::dit_kernel<<<256, 256, dit::L_BYTES, stream>>>((const u16*)d_in[0], (const u16*)d_in[1], (const u16*)d_in[2], (const u16*)d_in[3],
                                                      (const u16*)d_in[4], (const u16*)d_in[5], (const u16*)d_in[6], (u16*)d_out,
                                                      (char*)d_ws);
}
```

```cpp
#include <hip/hip_runtime.h>
#include <hip/hip_bf16.h>

typedef unsigned short u16;
typedef unsigned u32;
typedef unsigned long long u64;
typedef u32 v4u __attribute__((ext_vector_type(4)));
typedef u32 v2u __attribute__((ext_vector_type(2)));
typedef float f4 __attribute__((ext_vector_type(4)));
typedef float f2 __attribute__((ext_vector_type(2)));
typedef __bf16 b8 __attribute__((ext_vector_type(8)));
typedef __bf16 b2 __attribute__((ext_vector_type(2)));
typedef short s4 __attribute__((ext_vector_type(4)));
#define LAS __attribute__((address_space(3)))
#define GAS __attribute__((address_space(1)))
#define DEV __device__ __forceinline__
#define UNROLL _Pragma("unroll")
typedef __amdgpu_buffer_rsrc_t rsrc_t;

#define STAMP 0

namespace dit {

constexpr int SC1 = 16;
constexpr u32 SPIN_CAP = 1u << 16;
constexpr int OOB = 0x7ffffff0;

constexpr int WS_EPOCH = 0;
constexpr int WS_START = 1024;
constexpr int WS_RTF = 4096;
constexpr int WS_FLAG = 8192;
constexpr int WS_MOD = WS_FLAG + 256 * 512;
constexpr int WS_ML = WS_MOD + 2 * 2304 * 8;
constexpr int WS_ST = WS_ML + 256 * 512;
constexpr int WS_Q = WS_ST + 32 * 4096;
constexpr int WS_K = WS_Q + 64 * 24576;
constexpr int WS_V = WS_K + 64 * 24576;
constexpr int WS_OP = WS_V + 64 * 24576;
constexpr int WS_X1 = WS_OP + 256 * 24576;
constexpr int WS_HH = WS_X1 + 32 * 49152;
constexpr int WS_W1T = WS_HH + 32 * 196608;
constexpr int WS_W2T = WS_W1T + 64 * 39 * 1024;
constexpr int WS_END = WS_W2T + 16 * 72 * 1024;
constexpr int WS_STAMP = WS_END;
static_assert(WS_Q % 128 == 0 && WS_MOD % 128 == 0 && WS_ML % 128 == 0, "line alignment");

constexpr int L_WR = 0;
constexpr int L_Q = 0;
constexpr int L_P = 24576;
constexpr int L_MX = 57344;
constexpr int L_OC = 58368;
constexpr int L_GATE = 70656;
constexpr int L_STL = 73728;
constexpr int L_RED = 0;
constexpr int L_ACT = 110592;
constexpr int L_SC = L_ACT;
constexpr int L_MODA = 160768;
constexpr int L_BYTES = 163840;
#define K_W1P 16
#define K_W1B0 24
#define K_W1B 28
#define K_W1C0 34
#define K_W2C 1
#define K_W2W0 9
#define K_W2W 15
#define K_W2D0 21
#define K_W2D 57
#define K_W2E0 65
#define K_W2E 69
#define K_HDELAY 0
#define K_TOUCH 1
constexpr int W1_P = K_W1P, W1_B0 = K_W1B0, W1_B = K_W1B, W1_C0 = K_W1C0;
constexpr int W2_C = K_W2C, W2_W0 = K_W2W0, W2_W = K_W2W, W2_D0 = K_W2D0, W2_D = K_W2D, W2_E0 = K_W2E0, W2_E = K_W2E;
DEV constexpr bool w1_in_v(int n) { return n < 23; }
DEV constexpr bool w2_in_v(int n) { return n >= 48 && n < 58; }

#define ZERO4 (f4{0.f, 0.f, 0.f, 0.f})
#define FENCE() __builtin_amdgcn_sched_barrier(0)
DEV f4 mfma(v4u a, v4u b, f4 c) {
  return __builtin_amdgcn_mfma_f32_16x16x32_bf16(__builtin_bit_cast(b8, a), __builtin_bit_cast(b8, b), c, 0, 0, 0);
}
template <bool SWAP> DEV void mm(f4& c, const v4u& w, const v4u& x, bool w_in_v, bool first) {
  if (first) {
    if (w_in_v) { if (SWAP) asm volatile("v_mfma_f32_16x16x32_bf16 %0, %2, %1, 0" : "=&v"(c) : "v"(w), "v"(x));
                  else asm volatile("v_mfma_f32_16x16x32_bf16 %0, %1, %2, 0" : "=&v"(c) : "v"(w), "v"(x)); }
    else { if (SWAP) asm volatile("v_mfma_f32_16x16x32_bf16 %0, %2, %1, 0" : "=&v"(c) : "a"(w), "v"(x));
           else asm volatile("v_mfma_f32_16x16x32_bf16 %0, %1, %2, 0" : "=&v"(c) : "a"(w), "v"(x)); }
  } else {
    if (w_in_v) { if (SWAP) asm volatile("v_mfma_f32_16x16x32_bf16 %0, %2, %1, %0" : "+v"(c) : "v"(w), "v"(x));
                  else asm volatile("v_mfma_f32_16x16x32_bf16 %0, %1, %2, %0" : "+v"(c) : "v"(w), "v"(x)); }
    else { if (SWAP) asm volatile("v_mfma_f32_16x16x32_bf16 %0, %2, %1, %0" : "+v"(c) : "a"(w), "v"(x));
           else asm volatile("v_mfma_f32_16x16x32_bf16 %0, %1, %2, %0" : "+v"(c) : "a"(w), "v"(x)); }
  }
}
#define NOP8() do { FENCE(); asm volatile("s_nop 7"); FENCE(); } while (0)
DEV float asf(u32 v) { return __builtin_bit_cast(float, v); }
DEV u32 asu(float v) { return __builtin_bit_cast(u32, v); }
DEV u32 pk(float a, float b) { f2 v = {a, b}; return __builtin_bit_cast(u32, __builtin_convertvector(v, b2)); }
DEV float lo(u32 v) { return __builtin_bit_cast(float, v << 16); }
DEV float hi(u32 v) { return __builtin_bit_cast(float, v & 0xffff0000u); }
DEV float rbf(float v) { return lo(pk(v, v)); }
DEV float mx2(float a, float b) { return __builtin_elementwise_maximum(a, b); }
DEV float mx4(f4 a) { return mx2(mx2(mx2(a[0], a[1]), a[2]), a[3]); }
DEV void opq(float& a) { asm volatile("" : "+v"(a)); }
DEV void opq(f2& a) { asm volatile("" : "+v"(a)); }
DEV int vopq(int a) { asm volatile("" : "+v"(a)); return a; }
DEV int sopq(int a) { asm volatile("" : "+s"(a)); return a; }
DEV int mod3(int a) { return a >= 3 ? a - 3 : a; }
DEV rsrc_t mkrs(const void* p, int bytes) { return __builtin_amdgcn_make_buffer_rsrc((void*)p, 0, bytes, 0x00020000); }
template <int AUX> DEV v4u ld16(rsrc_t r, int v, int s) { return __builtin_amdgcn_raw_buffer_load_b128(r, v, s, AUX); }
template <int AUX> DEV v2u ld8(rsrc_t r, int v, int s) { return __builtin_amdgcn_raw_buffer_load_b64(r, v, s, AUX); }
template <int AUX> DEV void st16(v4u x, rsrc_t r, int v) { __builtin_amdgcn_raw_buffer_store_b128(x, r, v, 0, AUX); }
template <int AUX> DEV void st8(v2u x, rsrc_t r, int v) { __builtin_amdgcn_raw_buffer_store_b64(x, r, v, 0, AUX); }
#define LP(T, a) ((LAS T*)(size_t)(u32)(a))
DEV v4u lds16(int p) { return *LP(v4u, p); }
DEV f4 ldsf4(int p) { return *LP(f4, p); }
DEV void sts16(int p, v4u x) { *LP(v4u, p) = x; }
DEV v4u trfrag(int p, int rs4) {
  s4 a = __builtin_amdgcn_ds_read_tr16_b64_v4i16(LP(s4, p));
  s4 b = __builtin_amdgcn_ds_read_tr16_b64_v4i16(LP(s4, p + rs4));
  v2u x = __builtin_bit_cast(v2u, a), y = __builtin_bit_cast(v2u, b);
  return v4u{x.x, x.y, y.x, y.y};
}
DEV int fsw(int r) { return (r & 2) | ((r >> 1) & 4); }
DEV void bar() {
  asm volatile("s_waitcnt lgkmcnt(0)" ::: "memory");
  __builtin_amdgcn_s_barrier();
  asm volatile("" ::: "memory");
  FENCE();
}
template <int N> DEV void vmwait() { asm volatile("s_waitcnt vmcnt(%0)" :: "i"(N) : "memory"); }
DEV void dma3(u32 lds, int v0, int v1, int v2, rsrc_t r, int soff) {
  asm volatile("s_mov_b32 m0, %0\n\ts_nop 0\n\tbuffer_load_dwordx4 %1, %4, %5 offen lds\n\t"
               "buffer_load_dwordx4 %2, %4, %5 offen offset:1024 lds\n\tbuffer_load_dwordx4 %3, %4, %5 offen offset:2048 lds"
               :: "s"(lds), "v"(v0), "v"(v1), "v"(v2), "s"(r), "s"(soff) : "memory");
}
DEV void touch(int lds, rsrc_t rw, int voff) {
  asm volatile("s_mov_b32 m0, %0\n\ts_nop 0\n\tbuffer_load_dword %1, %2, 0 offen sc1 lds" :: "s"(lds), "v"(voff), "s"(rw) : "memory");
}
DEV bool same(v2u v, v2u tag) { return __all(v.x == tag.x && v.y == tag.y); }
DEV void poll_from(v2u v, rsrc_t rw, int voff, v2u tag) {
  if (!same(v, tag)) {
#pragma unroll 1
    for (u32 s = 0; s < SPIN_CAP; ++s) {
      asm volatile("" ::: "memory");
      if (same(ld8<SC1>(rw, voff, 0), tag)) break;
    }
  }
  asm volatile("" ::: "memory");
}
DEV void poll(rsrc_t rw, int voff, v2u tag) { poll_from(ld8<SC1>(rw, voff, 0), rw, voff, tag); }
template <int CTRL> DEV float dpp(float v) {
  return __builtin_bit_cast(float, __builtin_amdgcn_update_dpp(0, __builtin_bit_cast(int, v), CTRL, 0xf, 0xf, true));
}
DEV float xg_sum(float v) {
  auto a = __builtin_amdgcn_permlane16_swap(asu(v), asu(v), false, false);
  v = asf(a[0]) + asf(a[1]);
  auto b = __builtin_amdgcn_permlane32_swap(asu(v), asu(v), false, false);
  return asf(b[0]) + asf(b[1]);
}
DEV v4u pair_swap(v2u p0, v2u p1) {
  auto a = __builtin_amdgcn_permlane16_swap(p0.x, p1.x, false, false);
  auto b = __builtin_amdgcn_permlane16_swap(p0.y, p1.y, false, false);
  return v4u{a[0], b[0], a[1], b[1]};
}
DEV v2u pk4(f4 a) { return v2u{pk(a[0], a[1]), pk(a[2], a[3])}; }

DEV void mod_request(v2u (&v)[3], rsrc_t rw, int voff) {
  UNROLL for (int i = 0; i < 3; ++i) v[i] = ld8<SC1>(rw, voff, i * 2048);
}
template <bool ONE_B>
DEV void mod_finish(v2u (&v)[3], rsrc_t rw, int voff, int dst, v2u gtag, int tid, float mul) {
  auto ok = [&]() {
    return __all((((v[0].y ^ gtag.y) | (v[1].y ^ gtag.y) | (v[2].y ^ gtag.y)) |
                  (((v[0].x ^ gtag.x) | (v[1].x ^ gtag.x) | (v[2].x ^ gtag.x)) >> 16)) == 0);
  };
  if (!ok()) {
#pragma unroll 1
    for (u32 s = 0; s < SPIN_CAP; ++s) {
      asm volatile("" ::: "memory");
      mod_request(v, rw, voff);
      if (ok()) break;
    }
  }
  float f0 = lo(v[0].x), f1 = lo(v[1].x), f2_ = lo(v[2].x);
  if (ONE_B) { f1 = tid >= 128 ? rbf(1.f + f1) : f1; f2_ = rbf(1.f + f2_); }
  LAS float* d = LP(float, dst);
  d[0] = f0 * mul; d[256] = f1 * mul; d[512] = f2_ * mul;
}

#define TS_DECL()
#define TS(i) do {} while (0)

constexpr int RING = 4;
#define FRAG(n, so) ld16<SC1>(rw, l16[((n) % 12) / 4] + ((n) % 4) * 1024, (so)[(n) / 12])
#define FRESH_L16() UNROLL for (int j_ = 0; j_ < 3; ++j_) l16[j_] = vopq(l16[j_])

template <bool VTYPE, class F>
DEV void phaseA_gemm(const v4u (&wf)[12][3], const int (&ha)[12], rsrc_t rw, const int (&so)[3], F wodma) {
  auto hfrag = [&](int n) { return lds16(ha[(n % 24) >> 1] + ((n / 24) * 2 + (n & 1)) * 256); };
  f4 acc[2][2][3];
  auto epi = [&](int tp, int ft) {
    const v2u a = pk4(acc[tp][0][ft]), b = pk4(acc[tp][1][ft]);
    if (VTYPE) st16<SC1>(v4u{a.x, a.y, b.x, b.y}, rw, so[tp] + ft * 1024);
    else st16<SC1>(pair_swap(a, b), rw, so[ft] + tp * 512);
  };
  v4u ring[RING];
  UNROLL for (int n = 0; n < RING; ++n) ring[n] = hfrag(n);
  FENCE();
  UNROLL for (int n = 0; n < 48; ++n) {
    const int tp = n / 24, s = (n % 24) >> 1, t = n & 1;
    const v4u hb = ring[n % RING];
    mm<VTYPE>(acc[tp][t][0], wf[s][0], hb, s >= 9, s == 0);
    FENCE();
    if (n + RING < 48) ring[n % RING] = hfrag(n + RING);
    FENCE();
    mm<VTYPE>(acc[tp][t][1], wf[s][1], hb, s >= 9, s == 0);
    FENCE();
    if (n % 6 == 2 && n < 42) wodma(2 + n / 6);
    if (n == 27 || n == 31 || n == 35) epi(0, (n - 27) / 4);
    FENCE();
    mm<VTYPE>(acc[tp][t][2], wf[s][2], hb, s >= 9, s == 0);
    FENCE();
  }
  NOP8();
  UNROLL for (int ft = 0; ft < 3; ++ft) epi(1, ft);
}

__global__ void __launch_bounds__(256, 1)
dit_kernel(const u16* __restrict__ x, const u16* __restrict__ cvec, const u16* __restrict__ wqkv, const u16* __restrict__ wo,
           const u16* __restrict__ wmod, const u16* __restrict__ w1, const u16* __restrict__ w2, u16* __restrict__ out, char* ws) {
  extern __shared__ __attribute__((aligned(16))) char smem[];
  const int lb = sopq((int)(u32)(size_t)(LAS char*)smem);
  const int tid = threadIdx.x, lane = tid & 63, w = __builtin_amdgcn_readfirstlane(tid >> 6);
  const int i16 = lane & 15, g = lane >> 4;
  const int xcd = blockIdx.x & 7, idx = blockIdx.x >> 3;
  const int team = xcd * 4 + (idx >> 3), mem = idx & 7, bat = team >> 4, me = team * 8 + mem, tq = team & 3;
  TS_DECL();
  TS(0);
  {
    u64 pc; u32 len;
    asm volatile("s_getpc_b64 %0\ndit_code_begin:\n\ts_mov_b32 %1, dit_code_end - dit_code_begin" : "=s"(pc), "=s"(len));
    const rsrc_t rc = mkrs((const void*)(pc & ~127ull), len);
    const u32 per = (len / 32 + 127) & ~127u;
    if (w == 0) {
      const int off = lane * 128 < (int)per ? idx * per + lane * 128 : OOB;
      asm volatile("s_mov_b32 m0, %0\n\ts_nop 0\n\tbuffer_load_dword %1, %2, 0 offen lds" :: "s"(lb + L_MODA), "v"(off), "s"(rc) : "memory");
    }
  }
  asm volatile("" :: "s"(x), "s"(cvec), "s"(wqkv), "s"(wo), "s"(wmod), "s"(w1), "s"(w2), "s"(out), "s"(ws));
  TS(28);
  const rsrc_t rw = mkrs(ws, WS_END);
  const rsrc_t rx = mkrs(x, 2048 * 384 * 2);
  v2u tag, gtag;
  static_assert(WS_EPOCH == 0 && WS_FLAG == 0x2000, "offsets in EPOCH_ASK");
#define EPOCH_ASK() u64 ep_, tch0_, tch1_, tch2_, tch3_; \
  asm volatile("s_load_dwordx2 %0, %5, 0x0\n\ts_load_dwordx2 %1, %5, %6 offset:0x2000\n\ts_load_dwordx2 %2, %5, %6 offset:0x2080\n\t" \
               "s_load_dwordx2 %3, %5, %6 offset:0x2100\n\ts_load_dwordx2 %4, %5, %6 offset:0x2180" \
               : "=&s"(ep_), "=&s"(tch0_), "=&s"(tch1_), "=&s"(tch2_), "=&s"(tch3_) : "s"(ws), "s"(me * 512) : "memory")
#define EPOCH_WAIT() asm volatile("s_waitcnt lgkmcnt(0)" : "+s"(ep_), "+s"(tch0_), "+s"(tch1_), "+s"(tch2_), "+s"(tch3_) :: "memory"); \
  ep_ += 1; tag = v2u{(u32)ep_, (u32)(ep_ >> 32)}; gtag = v2u{(u32)ep_ << 16, (u32)(ep_ >> 16)}
  const int c_tt = mem >> 1, c_ch = mem & 1;
  const int r3 = xcd % 3;
  v4u wof[36];
  int pub, tra[3][3], woo[3];
  const rsrc_t rwo = mkrs(wo, 384 * 384 * 2);
  auto stage_off = [&](int (&o)[3], int stride) {
    UNROLL for (int i = 0; i < 3; ++i) {
      const int q = w * 192 + i * 64 + lane, row = q / 24, c = q % 24;
      o[i] = vopq(row * stride + (c ^ fsw(row)) * 16 - i * 1024);
    }
  };
  auto tr_setup = [&]() {
    UNROLL for (int ft = 0; ft < 3; ++ft) {
      const int row = 8 * g + (i16 >> 2), ch = 2 * (3 * w + ft) + ((lane & 3) >> 1);
      const int t = lb + row * 384 + (ch ^ fsw(row)) * 16 + 8 * (lane & 1);
      tra[0][ft] = vopq(t + L_ACT); tra[1][ft] = vopq(t + L_WR); tra[2][ft] = vopq(t + L_WR + 5 * 12288);
    }
  };
  auto wodma = [&](int s) {
    dma3(lb + (s < 9 ? L_WR + s * 12288 : L_ACT + (s - 9) * 12288) + w * 3072, woo[0], woo[1], woo[2], rwo,
         (mod3(s / 4 + r3) * 4 + (s & 3)) * 24576 + c_ch * 384);
  };

  if (mem < 6) {
    const rsrc_t rq = mkrs(wqkv, 384 * 1152 * 2);
    EPOCH_ASK();
    v4u xr[12];
    {
      const int xo = (g * 384 + i16 * 8) * 2;
      UNROLL for (int it = 0; it < 4; ++it) {
        const int so = (team * 64 + w * 16 + ((it + mem) & 3) * 4) * 768;
        asm volatile("buffer_load_dwordx4 %0, %3, %4, %5 offen\n\tbuffer_load_dwordx4 %1, %3, %4, %5 offen offset:256\n\t"
                     "buffer_load_dwordx4 %2, %3, %4, %5 offen offset:512"
                     : "=&v"(xr[3 * it]), "=&v"(xr[3 * it + 1]), "=&v"(xr[3 * it + 2]) : "v"(xo), "s"(rx), "s"(so) : "memory");
        if (it == 0)
          asm volatile("s_mov_b32 m0, %0\n\ts_nop 0\n\tbuffer_load_dword %1, %2, 0 offen lds"
                       :: "s"(lb + L_MODA + 256 + w * 256), "v"(lane < 9 ? (((team * 6 + mem) * 4 + w) * 9 + lane) * 128 : OOB), "s"(rq) : "memory");
      }
    }
    int woff[3];
    stage_off(woff, 2304);
    int ks = team % 12;
    int wso = ks * 73728 + mem * 384;
    const int hb0 = lb + L_ACT + g * 1024 + ((i16 ^ (((ks & 1) << 2) | g)) * 16), hb1 = hb0 ^ 64;
    int ha[12];
    auto wdma = [&](int s) {
      dma3(lb + (s < 3 ? L_ACT + s * 12288 : L_WR + (s - 3) * 12288) + w * 3072, woff[0], woff[1], woff[2], rq, wso);
      ha[s] = vopq(((s & 1) ? hb1 : hb0) + ks * 4096);
      ks += 1; wso += 73728;
      if (ks == 12) { ks = 0; wso -= 12 * 73728; }
      ks = sopq(ks); wso = sopq(wso);
      FENCE();
    };
    wdma(0); wdma(1); wdma(2);
    asm volatile("s_waitcnt vmcnt(9)"
                 : "+v"(xr[0]), "+v"(xr[1]), "+v"(xr[2]), "+v"(xr[3]), "+v"(xr[4]), "+v"(xr[5]), "+v"(xr[6]), "+v"(xr[7]), "+v"(xr[8]),
                   "+v"(xr[9]), "+v"(xr[10]), "+v"(xr[11]) :: "memory");
    EPOCH_WAIT();
    FENCE();
    TS(25);
    f2 xn[4][12];
    float st[4];
    UNROLL for (int it = 0; it < 4; ++it) {
      wdma(3 + it);
      f2 s = {0.f, 0.f};
      UNROLL for (int e = 0; e < 12; ++e) { xn[it][e] = f2{lo(xr[3 * it + e / 4][e & 3]), hi(xr[3 * it + e / 4][e & 3])}; s += xn[it][e]; }
      st[it] = s[0] + s[1];
      FENCE();
    }
#define SUM16X4() \
    UNROLL for (int it = 0; it < 4; ++it) st[it] += dpp<0x128>(st[it]); \
    UNROLL for (int it = 0; it < 4; ++it) st[it] += dpp<0x124>(st[it]); \
    UNROLL for (int it = 0; it < 4; ++it) st[it] += dpp<0x122>(st[it]); \
    UNROLL for (int it = 0; it < 4; ++it) st[it] += dpp<0x121>(st[it])
    wdma(7);
    SUM16X4();
    FENCE();
    UNROLL for (int it = 0; it < 4; ++it) {
      if (it & 1) wdma(8 + it / 2);
      const float mu = st[it] * (1.f / 384.f);
      f2 q = {0.f, 0.f};
      UNROLL for (int e = 0; e < 12; ++e) { xn[it][e] -= mu; q += xn[it][e] * xn[it][e]; }
      st[it] = q[0] + q[1];
      FENCE();
    }
    wdma(10);
    SUM16X4();
    UNROLL for (int it = 0; it < 4; ++it) st[it] = __builtin_amdgcn_rsqf(st[it] * (1.f / 384.f) + 1e-5f);
    FENCE();
    wdma(11);
    v2u mv[3];
    const int mo = vopq(WS_MOD + bat * 2304 * 8 + tid * 8);
    mod_request(mv, rw, mo);
    FENCE();
    UNROLL for (int it = 0; it < 4; ++it)
      UNROLL for (int e = 0; e < 12; ++e) { xn[it][e] *= st[it]; opq(xn[it][e]); }
    tr_setup();
    auto wtr = [&](int s, int ft) {
      return trfrag(s < 3 ? tra[0][ft] + s * 12288 : s < 8 ? tra[1][ft] + (s - 3) * 12288 : tra[2][ft] + (s - 8) * 12288, 4 * 384);
    };
    FENCE();
    TS(26);
    vmwait<9 + 3>();
    bar();
    v4u wf[12][3];
    UNROLL for (int s = 0; s < 9; ++s)
      UNROLL for (int ft = 0; ft < 3; ++ft) wf[s][ft] = wtr(s, ft);
    const int mb = vopq(lb + L_MODA + i16 * 32), md = vopq(lb + L_MODA + tid * 4);
    int hw[4], so[3];
    UNROLL for (int it = 0; it < 4; ++it) hw[it] = vopq(lb + L_ACT + i16 * 1024 + (((w * 16 + ((it + mem) & 3) * 4 + g) ^ (i16 & 7)) * 16));
    const int hd = mem & 1;
    UNROLL for (int j = 0; j < 3; ++j) {
      so[j] = vopq(mem >= 4 ? WS_V + (team * 2 + hd) * 24576 + (j * 12 + 3 * w) * 1024 + lane * 16
                            : (mem < 2 ? WS_Q : WS_K) + (team * 2 + hd) * 24576 + (6 * w + 2 * j + (g >> 1)) * 1024 + ((g & 1) * 16 + i16) * 16);
    }
    {
      const int nq = mem < 2 ? 4 : 16;
      const int base = mem < 2 ? (team * 8 + hd * 4) * 512 : (bat * 128 + hd * 4 + (xcd & 3)) * 512 + ((mem < 4 ? 1 : 5) + tq) * 8;
      pub = vopq(lane < nq ? WS_FLAG + base + lane * (mem < 2 ? 512 : 4096) : lane == 63 ? WS_START + (int)blockIdx.x * 8 : OOB);
    }
    stage_off(woo, 768);
    FENCE();
    TS(1);
    mod_finish<true>(mv, rw, mo, md, gtag, tid, mem < 2 ? 0.07216878364870322f * 1.4426950408889634f : 1.f);
    __builtin_amdgcn_s_waitcnt(0x0F70);
    bar();
    TS(2);
    f4 tg[3][4];
    UNROLL for (int i = 0; i < 3; ++i) {
      tg[i][0] = ldsf4(mb + i * 512); tg[i][1] = ldsf4(mb + i * 512 + 16);
      tg[i][2] = ldsf4(mb + i * 512 + 1536); tg[i][3] = ldsf4(mb + i * 512 + 1552);
    }
    FENCE();
    UNROLL for (int i = 0; i < 3; ++i) {
      if (i < 2) wodma(i);
      UNROLL for (int ft = 0; ft < 3; ++ft) wf[9 + i][ft] = wtr(9 + i, ft);
      const f2 tt[4] = {{tg[i][0][0], tg[i][0][1]}, {tg[i][0][2], tg[i][0][3]}, {tg[i][1][0], tg[i][1][1]}, {tg[i][1][2], tg[i][1][3]}};
      const f2 gg[4] = {{tg[i][2][0], tg[i][2][1]}, {tg[i][2][2], tg[i][2][3]}, {tg[i][3][0], tg[i][3][1]}, {tg[i][3][2], tg[i][3][3]}};
      UNROLL for (int it = 0; it < 4; ++it) {
        v4u h;
        UNROLL for (int e = 0; e < 4; ++e) { const f2 v = xn[it][i * 4 + e] * gg[e] + tt[e]; h[e] = pk(v[0], v[1]); }
        sts16(hw[it] + i * 16384, h);
      }
      FENCE();
    }
    bar();
    TS(3);
    if (mem >= 4) phaseA_gemm<true>(wf, ha, rw, so, wodma);
    else phaseA_gemm<false>(wf, ha, rw, so, wodma);
    TS(4);
    bar();
    wodma(9); wodma(10); wodma(11);
    FENCE();
  } else {
    const int hidx = team * 2 + (mem - 6);
    const int u2 = hidx < 12 ? hidx : hidx - 24;
    const int w2off = (u2 / 6) * 384 * 768 + (u2 % 6) * 128;
    const int yb = hidx >= 36, yu = hidx - 12 - yb * 24;
    const int srow = lane >> 3, sc = (lane & 7) ^ fsw(srow + 8 * w);
    const int trr = 8 * g + (i16 >> 2);
    const int trh = lb + L_WR + trr * 128 + ((2 * w + ((lane & 3) >> 1)) ^ fsw(trr)) * 16 + 8 * (lane & 1);
    auto unit_dma = [&](int buf, rsrc_t r, int stride, int soff) {
      const int v = (8 * w + srow) * stride + sc * 16;
      UNROLL for (int s = 0; s < 12; ++s)
        asm volatile("s_mov_b32 m0, %0\n\ts_nop 0\n\tbuffer_load_dwordx4 %1, %2, %3 offen lds"
                     :: "s"(lb + L_WR + buf * 49152 + s * 4096 + w * 1024), "v"(v), "s"(r), "s"(soff + s * 32 * stride) : "memory");
    };
    u32 cv[3];
    {
      const rsrc_t rc = mkrs(cvec, 768 * 2);
      const rsrc_t rX = mkrs(hidx < 36 ? wmod : w2, sopq(__builtin_amdgcn_readfirstlane(hidx < 36 ? 384 * 2304 * 2 : hidx < 48 ? 384 * 1536 * 2 : 0)));
      const rsrc_t rY = mkrs(w1, sopq(__builtin_amdgcn_readfirstlane(hidx >= 12 && hidx < 60 ? 384 * 1536 * 2 : 0)));
      if (K_HDELAY && hidx >= 12) __builtin_amdgcn_s_sleep(K_HDELAY);
      EPOCH_ASK();
      asm volatile("buffer_load_ushort %0, %3, %4, 0 offen\n\tbuffer_load_ushort %1, %3, %4, 0 offen offset:512\n\t"
                   "buffer_load_ushort %2, %3, %4, 0 offen offset:1024"
                   : "=&v"(cv[0]), "=&v"(cv[1]), "=&v"(cv[2]) : "v"(tid * 2), "s"(rc) : "memory");
      unit_dma(0, rX, hidx < 36 ? 4608 : 768, hidx < 36 ? hidx * 128 : w2off);
      unit_dma(1, rY, 3072, yu * 128);
      asm volatile("s_waitcnt vmcnt(24)" : "+v"(cv[0]), "+v"(cv[1]), "+v"(cv[2]) :: "memory");
      EPOCH_WAIT();
    }
    UNROLL for (int i = 0; i < 3; ++i) {
      const float c = lo(cv[i]);
      *LP(u16, lb + L_SC + (tid + 256 * i) * 2) = (u16)pk(c * __builtin_amdgcn_rcpf(1.f + __expf(-c)), 0.f);
    }
    auto w2_store = [&](const v4u (&f)[12]) {
      const int ft = 4 * (u2 % 6) + w;
      const int dst = lane * 16 + WS_W2T + (((ft / 6) * 4 + u2 / 6) * 72 + ft % 6) * 1024;
      UNROLL for (int s = 0; s < 12; ++s) st16<SC1>(f[s], rw, dst + s * 6144);
    };
    vmwait<12>();
    bar();
    if (hidx < 48) {
      v4u fb[12];
      UNROLL for (int s = 0; s < 12; ++s) fb[s] = trfrag(trh + s * 4096, 4 * 128);
      if (hidx < 36) {
        v4u fa[12];
        UNROLL for (int s = 0; s < 12; ++s) fa[s] = lds16(lb + L_SC + (i16 & 1) * 768 + s * 64 + g * 16);
        FENCE();
        f4 acc = ZERO4;
        UNROLL for (int s = 0; s < 12; ++s) acc = mfma(fa[s], fb[s], acc);
        const u32 p = pk(acc[0], acc[1]);
        const int ch = hidx / 6, pos = ch == 2 ? 4 : ch == 3 ? 2 : ch == 4 ? 3 : ch;
        const int go = g ? OOB : WS_MOD + (pos * 384 + (hidx % 6) * 64 + w * 16 + lane) * 8;
        st8<SC1>(v2u{gtag.x | (p & 0xffffu), gtag.y}, rw, go);
        st8<SC1>(v2u{gtag.x | (p >> 16), gtag.y}, rw, g ? OOB : go + 2304 * 8);
        FENCE();
        if (hidx < 12) unit_dma(1, mkrs(w2, 384 * 1536 * 2), 768, w2off);
      } else {
        w2_store(fb);
      }
    }
    TS(1);
    tr_setup();
    stage_off(woo, 768);
    vmwait<0>();
    bar();
    if (hidx < 60) {
      v2u mv[3];
      const int mo = vopq(WS_MOD + (yb * 2304 + 768) * 8 + tid * 8);
      v4u f[12];
      UNROLL for (int s = 0; s < 12; ++s) f[s] = trfrag(trh + 49152 + s * 4096, 4 * 128);
      if (hidx < 12) {
        w2_store(f);
        bar();
      } else {
        mod_request(mv, rw, mo);
        f2 wv[12][4];
        UNROLL for (int s = 0; s < 12; ++s)
          UNROLL for (int e = 0; e < 4; ++e) { wv[s][e] = f2{lo(f[s][e]), hi(f[s][e])}; opq(wv[s][e]); }
        mod_finish<true>(mv, rw, mo, lb + L_MODA + tid * 4, gtag, tid, 1.f);
        bar();
        TS(3);
        const int ft = 4 * yu + w;
        const int fdst = WS_W1T + ((yb * 32 + ft / 3) * 39 + ft % 3) * 1024 + lane * 16;
        const int gb = vopq(lb + L_MODA + g * 32);
        f4 gq[12][2], tq4[12][2];
        UNROLL for (int s = 0; s < 12; ++s) { gq[s][0] = ldsf4(gb + 1536 + s * 128); gq[s][1] = ldsf4(gb + 1552 + s * 128); }
        FENCE();
        v4u sc4[12];
        UNROLL for (int s = 0; s < 12; ++s) {
          UNROLL for (int e = 0; e < 4; ++e) {
            const f2 v = wv[s][e] * f2{gq[s][e >> 1][(e & 1) * 2], gq[s][e >> 1][(e & 1) * 2 + 1]};
            sc4[s][e] = pk(v[0], v[1]);
          }
          st16<SC1>(sc4[s], rw, fdst + s * 3072);
          tq4[s][0] = ldsf4(gb + s * 128); tq4[s][1] = ldsf4(gb + 16 + s * 128);
          FENCE();
        }
        const v4u ones = {0x3f803f80u, 0x3f803f80u, 0x3f803f80u, 0x3f803f80u};
        f4 au = ZERO4, av = ZERO4;
        UNROLL for (int s = 0; s < 12; ++s) {
          au = mfma(ones, sc4[s], au);
          av = mfma(v4u{pk(tq4[s][0][0], tq4[s][0][1]), pk(tq4[s][0][2], tq4[s][0][3]), pk(tq4[s][1][0], tq4[s][1][1]),
                        pk(tq4[s][1][2], tq4[s][1][3])}, f[s], av);
        }
        const float u = au[0], v = av[0], uh = rbf(u), vh = rbf(v);
        v4u e = {pk(uh, uh), pk(u - uh, vh), pk(vh, v - vh), 0u};
        if (g) e = v4u{0u, 0u, 0u, 0u};
        st16<SC1>(e, rw, fdst + 12 * 3072);
      }
    }
    FENCE();
    TS(2);
    UNROLL for (int s = 0; s < 12; ++s) wodma(s);
    pub = lane == 0 ? WS_START + (int)blockIdx.x * 8
        : lane == 1 && hidx >= 12 && hidx < 60 ? WS_RTF + (8 + hidx - 12) * 8
        : lane == 2 && (hidx < 12 || (hidx >= 36 && hidx < 48)) ? WS_RTF + (64 + u2) * 8 : OOB;
    TS(4);
    __builtin_amdgcn_s_waitcnt(0xC07F);
    FENCE();
  }

  v4u w1f[39];
  int w1s[4], l16[3];
  UNROLL for (int j = 0; j < 4; ++j) w1s[j] = sopq(WS_W1T + (bat * 32 + mem * 4 + w) * 39936 + (j < 3 ? mod3(j + r3) : 3) * 12288);
  UNROLL for (int j = 0; j < 3; ++j) l16[j] = vopq(lane * 16 + j * 4096);
  const int rt_w1 = vopq(WS_RTF + (8 + bat * 24 + (lane < 24 ? lane : 23)) * 8);
  const int hd = mem >> 2, sp = mem & 3;
  const int fl = vopq(WS_FLAG + me * 512 + (lane < 8 ? lane : 7) * 8);
  const int fl0 = vopq(WS_FLAG + me * 512 + (lane < 9 ? lane : 8) * 8);
  const int fp = vopq(lane < 8 ? WS_FLAG + (team * 8 + lane) * 512 + mem * 8 : OOB);
  int kso[3], vso[4], qa[3], pa[4], fo2[2], vv2[2];
  {
    const int kt0 = bat * 16 + sp * 4;
    UNROLL for (int j = 0; j < 3; ++j) {
      kso[j] = sopq(WS_K + ((kt0 + w) * 2 + hd) * 24576 + mod3(j + r3) * 8192);
      qa[j] = vopq(lb + L_Q + mod3(j + r3) * 8192 + g * 1024 + i16 * 16);
    }
    UNROLL for (int j = 0; j < 4; ++j) {
      vso[j] = sopq(WS_V + ((kt0 + ((j + xcd) & 3)) * 2 + hd) * 24576 + 3 * w * 1024);
      pa[j] = vopq(lb + L_P + ((j + xcd) & 3) * 8192 + lane * 16);
    }
    UNROLL for (int j = 0; j < 2; ++j) { fo2[j] = vopq(g * 1024 + i16 * 16 + j * 4096); vv2[j] = vopq(lane * 16 + j * 12288); }
  }
  const int t16 = vopq(tid * 16), qw = vopq(lb + L_Q + tid * 16);
  const int qso = sopq(WS_Q + (team * 2 + hd) * 24576);
  const int pst = (g >> 1) * 1024 + ((g & 1) * 16 + i16) * 16;
  const int mxa = vopq(lb + L_MX + i16 * 16), mxw = vopq(lb + L_MX + lane * 16 + w * 4), mxo = vopq(lb + L_MX + (w * 16 + i16) * 16);
  const int pw = vopq(lb + L_P + w * 8192 + lane * 16), lso = vopq(lb + L_P + w * 1024 + lane * 16);
  int oso[3];
  UNROLL for (int dt = 0; dt < 3; ++dt) oso[dt] = vopq(WS_OP + me * 24576 + (6 * w + 2 * dt) * 1024 + pst);
  const int mlo = vopq(g ? OOB : WS_ML + me * 512 + w * 128 + i16 * 8);
  const int rt_w2 = vopq(WS_RTF + (64 + (lane < 24 ? lane : 23)) * 8);
  FENCE();
  vmwait<9>();
  bar();
  v2u fv;
  if (w == 0) { st8<SC1>(tag, rw, pub); fv = ld8<SC1>(rw, fl0, 0); }
  TS(5);
  auto wotr = [&](int s, int ft) {
    return trfrag(s < 5 ? tra[1][ft] + s * 12288 : s < 9 ? tra[2][ft] + (s - 5) * 12288 : tra[0][ft] + (s - 9) * 12288, 4 * 384);
  };
  UNROLL for (int n = 0; n < 27; ++n) wof[n] = wotr(n / 3, n % 3);
  FENCE();
  if (w == 0) poll_from(fv, rw, fl0, tag);
  vmwait<0>();
  bar();
  TS(7);

  const int e_th = mem & 1, e_cs = mem >> 1;
  v4u w2f[72];
  {
    v4u qs[6];
    UNROLL for (int i = 0; i < 6; ++i) qs[i] = ld16<SC1>(rw, t16, qso + i * 4096);
    v4u kf[6][4], vf[8][3];
    auto kfrag = [&](int s, int kt) { kf[s][kt] = ld16<SC1>(rw, fo2[s & 1] + kt * 256, kso[s >> 1]); };
    auto vfrag = [&](int v) { vf[v / 3][v % 3] = ld16<SC1>(rw, vv2[(v / 3) & 1] + (v % 3) * 1024, vso[v / 6]); };
    UNROLL for (int n = 0; n < 8; ++n) kfrag(n >> 2, n & 3);
    FENCE();
    UNROLL for (int n = 27; n < 36; ++n) wof[n] = wotr(n / 3, n % 3);
    FENCE();
    UNROLL for (int i = 0; i < 6; ++i) sts16(qw + i * 4096, qs[i]);
    FENCE();
    UNROLL for (int n = 0; n < 4; ++n) kfrag(2, n);
    bar();
    f4 sa[4][4];
    float m[4];
    auto rowmax = [&](int qt) {
      float a = mx2(mx2(sa[0][qt][0], sa[0][qt][1]), sa[0][qt][2]), b = mx2(mx2(sa[0][qt][3], sa[1][qt][0]), sa[1][qt][1]);
      a = mx2(mx2(a, sa[1][qt][2]), sa[1][qt][3]); b = mx2(mx2(b, sa[2][qt][0]), sa[2][qt][1]);
      a = mx2(mx2(a, sa[2][qt][2]), sa[2][qt][3]); b = mx2(mx2(b, sa[3][qt][0]), sa[3][qt][1]);
      m[qt] = mx2(mx2(mx2(a, sa[3][qt][2]), sa[3][qt][3]), b);
      opq(m[qt]);
    };
    {
      auto qfrag = [&](int n) { return lds16(qa[n >> 3] + ((n >> 2) & 1) * 4096 + (n & 3) * 256); };
      v4u ring[RING];
      UNROLL for (int n = 0; n < RING; ++n) ring[n] = qfrag(n);
      FENCE();
      UNROLL for (int n = 0; n < 24; ++n) {
        const int s = n >> 2, qt = n & 3;
        const v4u qb = ring[n % RING];
        mm<false>(sa[0][qt], kf[s][0], qb, false, s == 0);
        FENCE();
        if (n + RING < 24) ring[n % RING] = qfrag(n + RING);
        FENCE();
        mm<false>(sa[1][qt], kf[s][1], qb, false, s == 0);
        FENCE();
        if (s < 3) kfrag(s + 3, qt); else vfrag(n - 12);
        FENCE();
        mm<false>(sa[2][qt], kf[s][2], qb, false, s == 0);
        mm<false>(sa[3][qt], kf[s][3], qb, false, s == 0);
        FENCE();
        if (n == 23) { rowmax(0); rowmax(1); }
        FENCE();
      }
    }
    TS(8);
    vfrag(12);
    FENCE();
    rowmax(2);
    vfrag(13);
    FENCE();
    rowmax(3);
    FENCE();
    {
      auto a = __builtin_amdgcn_permlane16_swap(asu(m[0]), asu(m[1]), false, false);
      auto b = __builtin_amdgcn_permlane16_swap(asu(m[2]), asu(m[3]), false, false);
      const float p = mx2(asf(a[0]), asf(a[1])), q = mx2(asf(b[0]), asf(b[1]));
      auto c = __builtin_amdgcn_permlane32_swap(asu(p), asu(q), false, false);
      *LP(float, mxw) = mx2(asf(c[0]), asf(c[1]));
    }
    vfrag(14); vfrag(15);
    v2u rtv = ld8<SC1>(rw, rt_w1, 0);
    bar();
    f4 m4[4];
    UNROLL for (int qt = 0; qt < 4; ++qt) m4[qt] = ldsf4(mxa + qt * 256);
    const f4 mo4 = ldsf4(mxo);
    FENCE();
    UNROLL for (int qt = 0; qt < 4; ++qt) {
      const float mq = mx4(m4[qt]);
      UNROLL for (int u = 0; u < 2; ++u) {
        vfrag(16 + 2 * qt + u);
        FENCE();
        const f4 a = sa[2 * u][qt] - mq, b = sa[2 * u + 1][qt] - mq;
        sts16(pw + (u * 4 + qt) * 1024,
              v4u{pk(__builtin_amdgcn_exp2f(a[0]), __builtin_amdgcn_exp2f(a[1])), pk(__builtin_amdgcn_exp2f(a[2]), __builtin_amdgcn_exp2f(a[3])),
                  pk(__builtin_amdgcn_exp2f(b[0]), __builtin_amdgcn_exp2f(b[1])), pk(__builtin_amdgcn_exp2f(b[2]), __builtin_amdgcn_exp2f(b[3]))});
        FENCE();
      }
    }
    float mw = mx4(mo4);
    opq(mw);
    v4u ones = {0x3f803f80u, 0x3f803f80u, 0x3f803f80u, 0x3f803f80u};
    asm volatile("" : "+v"(ones));
    poll_from(rtv, rw, rt_w1, tag);
    FRESH_L16();
    bar();
    TS(9);
    f4 lsum;
    f4 oa[2][2][3];
    auto oepi = [&](int tp, int dt) { st16<SC1>(pair_swap(pk4(oa[tp][0][dt]), pk4(oa[tp][1][dt])), rw, oso[dt] + tp * 512); };
    auto pfrag = [&](int n) {
      return lds16(pa[(n & 15) >> 2] + ((n >> 1) & 1) * 4096 + (2 * (n >> 4) + (n & 1)) * 1024);
    };
    v4u ring[RING], lsf[2];
    UNROLL for (int n = 0; n < RING; ++n) ring[n] = pfrag(n);
    FENCE();
    UNROLL for (int n = 0; n < 32; ++n) {
      const int tp = n >> 4, s = (n & 15) >> 1, t = n & 1;
      const v4u pb = ring[n % RING];
      mm<false>(oa[tp][t][0], vf[s][0], pb, false, s == 0);
      FENCE();
      if (n + RING < 32) ring[n % RING] = pfrag(n + RING);
      FENCE();
      mm<false>(oa[tp][t][1], vf[s][1], pb, false, s == 0);
      FENCE();
      if ((n & 1) == 0 && n / 2 < W1_P) w1f[n / 2] = FRAG(n / 2, w1s);
      if (n == 19 || n == 23 || n == 27) oepi(0, (n - 19) / 4);
      if (n == 25) { asm volatile("" : "+v"(lsum)); st8<SC1>(v2u{asu(mw), asu(lsum[0])}, rw, mlo); }
      FENCE();
      mm<false>(oa[tp][t][2], vf[s][2], pb, false, s == 0);
      if ((n & 1) && n >= 5 && n < 21) mm<false>(lsum, ones, lsf[(n >> 1) & 1], true, n == 5);
      FENCE();
      if ((n & 1) && n < 17) lsf[(n >> 1) & 1] = lds16(lso + (n >> 1) * 4096);
      FENCE();
    }
    NOP8();
    UNROLL for (int dt = 0; dt < 3; ++dt) oepi(1, dt);
    TS(10);
  }
  FENCE();
  v2u gv[3], xres[3];
  const int go = vopq(WS_MOD + (bat * 2304 + 1536) * 8 + tid * 8);
  mod_request(gv, rw, go);
  const v2u rt2 = ld8<SC1>(rw, rt_w2, 0);
  UNROLL for (int n = W1_P; n < W1_B0; ++n) w1f[n] = FRAG(n, w1s);
  FENCE();
  int w2s[6], oca[3], ops[4];
  UNROLL for (int j = 0; j < 6; ++j) w2s[j] = sopq(WS_W2T + (e_cs * 4 + w) * 73728 + mod3(j / 2 + r3) * 24576 + (j & 1) * 12288);
  UNROLL for (int j = 0; j < 3; ++j) oca[j] = vopq(lb + L_OC + mod3(j + r3) * 4096 + lane * 16);
  UNROLL for (int s = 0; s < 4; ++s) ops[s] = sopq(WS_OP + (team * 8 + (w >> 1) * 4 + s) * 24576 + (w & 1) * 12288 + c_tt * 256);
  const int fo3 = vopq(g * 1024 + i16 * 16 + 8192);
  const int mla = vopq(WS_ML + (team * 8 + (w >> 1) * 4) * 512 + c_tt * 128 + i16 * 8);
  const int ocw = vopq(lb + L_OC + 3 * w * 1024 + lane * 16);
  const int gaa = vopq(lb + L_GATE + (c_ch * 192 + w * 48 + g * 4) * 4), gd = vopq(lb + L_GATE + tid * 4);
  const int xd = WS_X1 + team * 49152 + (c_ch * 24 + w * 6) * 1024 + c_tt * 256 + i16 * 16;
  const int xd0 = vopq(xd + ((g & 1) * 2 + (g >> 1)) * 1024), xd1 = vopq((g & 1) ? OOB : xd + (4 + (g >> 1)) * 1024);
  const int sto = vopq(g > 1 ? OOB : WS_ST + team * 4096 + (c_tt * 8 + c_ch * 4 + w) * 128 + lane * 4);
  FENCE();
  const int xo = vopq(i16 * 768 + g * 8);
  FENCE();
  vmwait<4 + W1_B0 - W1_P>();
  bar();
  if (w == 0) { st8<SC1>(tag, rw, fp + 128); fv = ld8<SC1>(rw, fl + 128, 0); }
  TS(11);
  FRESH_L16();
  UNROLL for (int n = W1_B0; n < W1_B; ++n) w1f[n] = FRAG(n, w1s);
  UNROLL for (int ft = 0; ft < 3; ++ft) xres[ft] = ld8<0>(rx, xo + ft * 32, (team * 64 + c_tt * 16) * 768 + (c_ch * 192 + w * 48) * 2);
  FENCE();
  poll_from(rt2, rw, rt_w2, tag);
  mod_finish<false>(gv, rw, go, gd, gtag, tid, 1.f);
  if (w == 0) poll_from(fv, rw, fl + 128, tag);
  bar();
  TS(13);

  {
    FRESH_L16();
    v2u ml[4];
    UNROLL for (int s = 0; s < 4; ++s) ml[s] = ld8<SC1>(rw, mla + s * 512, 0);
    v4u op[3][4];
    UNROLL for (int i = 0; i < 3; ++i)
      UNROLL for (int s = 0; s < 4; ++s) op[i][s] = ld16<SC1>(rw, i < 2 ? fo2[i] : fo3, ops[s]);
    FENCE();
    float wt[4];
    {
      const float m = mx2(mx2(asf(ml[0].x), asf(ml[1].x)), mx2(asf(ml[2].x), asf(ml[3].x)));
      UNROLL for (int s = 0; s < 4; ++s) wt[s] = __builtin_amdgcn_exp2f(asf(ml[s].x) - m);
      const float inv = __builtin_amdgcn_rcpf((wt[0] * asf(ml[0].y) + wt[1] * asf(ml[1].y)) + (wt[2] * asf(ml[2].y) + wt[3] * asf(ml[3].y)));
      UNROLL for (int s = 0; s < 4; ++s) wt[s] *= inv;
    }
    FENCE();
    UNROLL for (int i = 0; i < 3; ++i) {
      if (i > 0) { UNROLL for (int n = W1_B + (i - 1) * (W1_C0 - W1_B) / 2; n < W1_B + i * (W1_C0 - W1_B) / 2; ++n) w1f[n] = FRAG(n, w1s); }
      FENCE();
      v4u o;
      UNROLL for (int e = 0; e < 4; ++e) {
        f2 a = f2{lo(op[i][0][e]), hi(op[i][0][e])} * wt[0];
        UNROLL for (int s = 1; s < 4; ++s) a += f2{lo(op[i][s][e]), hi(op[i][s][e])} * wt[s];
        o[e] = pk(a[0], a[1]);
      }
      sts16(ocw + i * 1024, o);
      FENCE();
    }
    bar();
    TS(27);
    v4u ob[12];
    UNROLL for (int s = 0; s < 12; ++s) ob[s] = lds16(oca[s >> 2] + (s & 3) * 1024);
    f4 ga[3], xf[3];
    UNROLL for (int ft = 0; ft < 3; ++ft) ga[ft] = ldsf4(gaa + ft * 64);
    FENCE();
    f4 acc[3];
    UNROLL for (int s = 0; s < 12; ++s) {
      mm<false>(acc[0], wof[s * 3], ob[s], false, s == 0);
      FENCE();
      if ((s & 1) == 0) {
        const int n = W1_C0 + s / 2;
        if (n < 39) w1f[n] = FRAG(n, w1s); else if (n - 39 < W2_C) w2f[n - 39] = FRAG(n - 39, w2s);
      }
      if (s >= 3 && s < 9 && (s & 1)) {
        const int ft = (s - 3) / 2;
        xf[ft] = f4{lo(xres[ft].x), hi(xres[ft].x), lo(xres[ft].y), hi(xres[ft].y)};
        asm volatile("" : "+v"(xf[ft]));
      }
      FENCE();
      mm<false>(acc[1], wof[s * 3 + 1], ob[s], false, s == 0);
      mm<false>(acc[2], wof[s * 3 + 2], ob[s], false, s == 0);
      FENCE();
    }
    NOP8();
    v2u xp[3];
    UNROLL for (int ft = 0; ft < 3; ++ft) xp[ft] = pk4(xf[ft] + ga[ft] * acc[ft]);
    st16<SC1>(pair_swap(xp[0], xp[1]), rw, xd0);
    st16<SC1>(pair_swap(xp[2], xp[2]), rw, xd1);
    FENCE();
    f2 s1 = {0.f, 0.f}, s2 = {0.f, 0.f};
    UNROLL for (int ft = 0; ft < 3; ++ft) {
      const f2 a = {lo(xp[ft].x), hi(xp[ft].x)}, b = {lo(xp[ft].y), hi(xp[ft].y)};
      s1 += a + b;
      s2 += a * a + b * b;
    }
    {
      auto a = __builtin_amdgcn_permlane16_swap(asu(s1[0] + s1[1]), asu(s2[0] + s2[1]), false, false);
      const float p = asf(a[0]) + asf(a[1]);
      auto b = __builtin_amdgcn_permlane32_swap(asu(p), asu(p), false, false);
      __builtin_amdgcn_raw_buffer_store_b32(asu(asf(b[0]) + asf(b[1])), rw, sto, 0, SC1);
    }
    TS(14);
  }
  FENCE();
  UNROLL for (int n = W2_C; n < W2_W0; ++n) w2f[n] = FRAG(n, w2s);
  FENCE();
  int xa[4], hso[3];
  UNROLL for (int j = 0; j < 3; ++j) {
    xa[j] = vopq(lb + L_ACT + mod3(j + r3) * 16384 + g * 1024 + i16 * 16);
    hso[j] = vopq(WS_HH + team * 196608 + (mem * 24 + w * 6 + 2 * j) * 1024 + pst);
  }
  xa[3] = vopq(lb + L_ACT + 49152 + i16 * 16);
  const int xw = vopq(lb + L_ACT + tid * 16);
  const int x1s = sopq(WS_X1 + team * 49152);
  const int sta = vopq(WS_ST + team * 4096 + w * 1024 + g * 256 + i16 * 4);
  const int stw = vopq(lb + L_STL + (w * 16 + i16) * 16), ste = vopq(lb + L_ACT + 49152 + (w * 16 + i16) * 16), kka = vopq(lb + L_STL + i16 * 16);
  FENCE();
  const int tx = vopq(lane < 48 ? WS_X1 + team * 49152 + (c_ch * 24 + (lane >> 1)) * 1024 + c_tt * 256 + (lane & 1) * 128
                    : lane < 52 ? WS_ST + team * 4096 + (c_tt * 8 + c_ch * 4 + lane - 48) * 128 : OOB);
  FENCE();
  vmwait<W2_W0 - W2_C>();
  bar();
  if (w == 0) { st8<SC1>(tag, rw, fp + 256); fv = ld8<SC1>(rw, fl + 256, 0); }
  else if (K_TOUCH && w == 1) touch(lb + L_MODA + 256, rw, tx);
  TS(15);
  FRESH_L16();
  UNROLL for (int n = W2_W0; n < W2_W; ++n) w2f[n] = FRAG(n, w2s);
  FENCE();
  if (w == 0) poll_from(fv, rw, fl + 256, tag);
  if (w == 2 && blockIdx.x == 0) {
    v2u sv[4];
    UNROLL for (int i = 0; i < 4; ++i) sv[i] = ld8<SC1>(rw, WS_START + lane * 8 + i * 512, 0);
    UNROLL for (int i = 0; i < 4; ++i) poll_from(sv[i], rw, WS_START + lane * 8 + i * 512, tag);
    st8<SC1>(tag, rw, lane ? OOB : WS_EPOCH);
  }
  bar();
  TS(17);

  {
    FRESH_L16();
    u32 stv[4];
    UNROLL for (int e = 0; e < 4; ++e) stv[e] = __builtin_amdgcn_raw_buffer_load_b32(rw, sta + e * 64, 0, SC1);
    v4u xs[12];
    UNROLL for (int i = 0; i < 12; ++i) xs[i] = ld16<SC1>(rw, t16, x1s + i * 4096);
    FENCE();
    {
      const float mu = xg_sum(asf(stv[0]) + asf(stv[2])) * (1.f / 384.f);
      const float var = fmaxf(xg_sum(asf(stv[1]) + asf(stv[3])) * (1.f / 384.f) - mu * mu, 0.f) + 1e-5f;
      const float r = __builtin_amdgcn_rsqf(var), sg = var * r;
      const float mh = rbf(-mu), sh = rbf(sg);
      *LP(f4, stw) = f4{-2.30220819f * r, -0.10294324f * r * r * r, r, 0.f};
      sts16(ste, v4u{pk(mh, -mu - mh), pk(mh, sh), pk(sg - sh, sh), 0u});
    }
    FENCE();
    UNROLL for (int i = 0; i < 12; ++i) {
      sts16(xw + i * 4096, xs[i]);
      if (i >= 6 && W2_W + i - 6 < W2_D0) w2f[W2_W + i - 6] = FRAG(W2_W + i - 6, w2s);
      FENCE();
    }
    bar();
    TS(18);
    auto xfrag = [&](int s, int t) { return lds16(xa[s >> 2] + (s & 3) * 4096 + t * 256); };
    v4u xb[13];
    UNROLL for (int s = 0; s < 13; ++s) xb[s] = xfrag(s, 0);
    f2 kk[4];
    UNROLL for (int t = 0; t < 4; ++t) kk[t] = *LP(f2, kka + t * 256);
    FENCE();
    f4 acc[2];
    float y[4];
    v2u hp[2][3];
    auto stage = [&](int q, int j) {
      const int t = q / 3, ft = q % 3;
      const f4& z = acc[q & 1];
      if (j == 0) { UNROLL for (int e = 0; e < 4; ++e) { y[e] = z[e] * z[e]; opq(y[e]); } }
      if (j == 1) { UNROLL for (int e = 0; e < 4; ++e) { y[e] = y[e] * kk[t][1] + kk[t][0]; opq(y[e]); } }
      if (j == 2) { UNROLL for (int e = 0; e < 4; ++e) { y[e] = y[e] * z[e]; opq(y[e]); } }
      if (j == 3 || j == 4) { UNROLL for (int e = 2 * j - 6; e < 2 * j - 4; ++e) { y[e] = __builtin_amdgcn_exp2f(y[e]); opq(y[e]); } }
      if (j == 5) { UNROLL for (int e = 0; e < 4; ++e) { y[e] = y[e] + 1.f; opq(y[e]); } }
      if (j == 6 || j == 7) { UNROLL for (int e = 2 * j - 12; e < 2 * j - 10; ++e) { y[e] = __builtin_amdgcn_rcpf(y[e]); opq(y[e]); } }
      if (j == 8) { UNROLL for (int e = 0; e < 4; ++e) { y[e] = y[e] * z[e]; opq(y[e]); } }
      if (j == 9) { hp[t & 1][ft] = v2u{pk(y[0], y[1]), pk(y[2], y[3])}; asm volatile("" : "+v"(hp[t & 1][ft])); }
      if (j == 10 && (t & 1)) st16<SC1>(pair_swap(hp[0][ft], hp[1][ft]), rw, hso[ft] + (t >> 1) * 512);
    };
    UNROLL for (int q = 0; q < 12; ++q) {
      const int t = q / 3, ft = q % 3;
      UNROLL for (int s = 0; s < 13; ++s) {
        mm<false>(acc[q & 1], w1f[s * 3 + ft], xb[s], w1_in_v(s * 3 + ft), s == 0);
        FENCE();
        if (ft == 2 && t < 3) xb[s] = xfrag(s, t + 1);
        if (s < 3 && W2_D0 + 3 * q + s < W2_D) w2f[W2_D0 + 3 * q + s] = FRAG(W2_D0 + 3 * q + s, w2s);
        if (q > 1 && s < 2) stage(q - 2, 9 + s);
        if (q > 0 && s == 3) asm volatile("s_nop 1");
        if (q > 0 && s >= 4) stage(q - 1, s - 4);
        FENCE();
      }
    }
    stage(10, 9); stage(10, 10);
    NOP8();
    UNROLL for (int j = 0; j < 11; ++j) stage(11, j);
    TS(19);
  }
  FENCE();
  UNROLL for (int n = W2_D; n < W2_E0; ++n) w2f[n] = FRAG(n, w2s);
  FENCE();
  v4u x1r[2];
  f4 gm[2][2];
  int ra[2], oo[2], hv[4], hs[3];
  UNROLL for (int i = 0; i < 2; ++i) {
    const int n = tid + 256 * i, nn = n < 384 ? n : 383, tok = nn / 12, c8 = nn % 12;
    x1r[i] = lds16(lb + L_ACT + (e_cs * 12 + c8) * 1024 + (e_th * 32 + tok) * 16);
    const float r = *LP(float, lb + L_STL + (e_th * 32 + tok) * 16 + 8);
    gm[i][0] = ldsf4(lb + L_GATE + 1536 + (e_cs * 96 + c8 * 8) * 4) * r;
    gm[i][1] = ldsf4(lb + L_GATE + 1536 + (e_cs * 96 + c8 * 8) * 4 + 16) * r;
    asm volatile("" : "+v"(gm[i][0]), "+v"(gm[i][1]), "+v"(x1r[i]));
    ra[i] = vopq(lb + L_RED + tok * 400 + c8 * 32);
    oo[i] = vopq(n < 384 ? (team * 64 + e_th * 32 + tok) * 768 + e_cs * 192 + c8 * 16 : OOB);
  }
  UNROLL for (int j = 0; j < 4; ++j) hv[j] = vopq(g * 1024 + i16 * 16 + j * 4096);
  UNROLL for (int j = 0; j < 3; ++j) hs[j] = sopq(WS_HH + team * 196608 + w * 49152 + e_th * 512 + mod3(j + r3) * 16384);
  const int rdw = vopq(lb + L_RED + w * 12800 + i16 * 400 + g * 16);
  const rsrc_t ro = mkrs(out, 2048 * 384 * 2);
  FENCE();
  vmwait<W2_E0 - W2_D>();
  bar();
  if (w == 0) { st8<SC1>(tag, rw, fp + 384); fv = ld8<SC1>(rw, fl + 384, 0); }
  TS(20);
  FRESH_L16();
  UNROLL for (int n = W2_E0; n < W2_E; ++n) w2f[n] = FRAG(n, w2s);
  FENCE();
  if (w == 0) poll_from(fv, rw, fl + 384, tag);
  bar();
  TS(22);

  {
    v4u hb[12][2];
    UNROLL for (int j = 0; j < 4; ++j) hv[j] = vopq(hv[j]);
    FRESH_L16();
    auto hload = [&](int n) { hb[n >> 1][n & 1] = ld16<SC1>(rw, hv[(n >> 1) & 3] + (n & 1) * 256, hs[n >> 3]); };
    UNROLL for (int n = 0; n < 10; ++n) hload(n);
    FENCE();
    f4 acc[2][6];
    UNROLL for (int n = 0; n < 24; ++n) {
      const int s = n >> 1, t = n & 1;
      mm<false>(acc[t][0], w2f[s * 6], hb[s][t], w2_in_v(s * 6), s == 0);
      FENCE();
      if (n + 10 < 24) hload(n + 10);
      else if (W2_E + n - 14 < 72) w2f[W2_E + n - 14] = FRAG(W2_E + n - 14, w2s);
      FENCE();
      UNROLL for (int ft = 1; ft < 6; ++ft) {
        mm<false>(acc[t][ft], w2f[s * 6 + ft], hb[s][t], w2_in_v(s * 6 + ft), s == 0);
        if (n == 23 && ft >= 3) { FENCE(); *LP(f4, rdw + (ft - 3) * 64) = acc[0][ft - 3]; FENCE(); }
      }
      FENCE();
    }
    UNROLL for (int ft = 3; ft < 6; ++ft) *LP(f4, rdw + ft * 64) = acc[0][ft];
    FENCE();
    asm volatile("s_nop 3");
    FENCE();
    UNROLL for (int ft = 0; ft < 6; ++ft) { *LP(f4, rdw + 6400 + ft * 64) = acc[1][ft]; FENCE(); }
    bar();
    TS(23);
    UNROLL for (int i = 0; i < 2; ++i) {
      if (i == 0 || w < 2) {
        f4 pa_[4], pb_[4];
        UNROLL for (int q = 0; q < 4; ++q) { pa_[q] = ldsf4(ra[i] + q * 12800); pb_[q] = ldsf4(ra[i] + q * 12800 + 16); }
        FENCE();
        const f4 a = (pa_[0] + pa_[1]) + (pa_[2] + pa_[3]), b = (pb_[0] + pb_[1]) + (pb_[2] + pb_[3]);
        const v4u xr = x1r[i];
        const f4 g0 = gm[i][0], g1 = gm[i][1];
        const v4u o = {pk(lo(xr.x) + g0[0] * a[0], hi(xr.x) + g0[1] * a[1]), pk(lo(xr.y) + g0[2] * a[2], hi(xr.y) + g0[3] * a[3]),
                       pk(lo(xr.z) + g1[0] * b[0], hi(xr.z) + g1[1] * b[1]), pk(lo(xr.w) + g1[2] * b[2], hi(xr.w) + g1[3] * b[3])};
        st16<SC1>(o, ro, oo[i]);
      }
    }
    TS(24);
  }
  asm volatile("dit_code_end:");
}

}

extern "C" void kernel_launch(void* const* d_in, const int* in_sizes, int n_in, void* d_out, int out_size, void* d_ws,
                              size_t ws_size, hipStream_t stream) {
  (void)in_sizes; (void)n_in; (void)out_size; (void)ws_size;
  (void)hipFuncSetAttribute((const void*)dit::dit_kernel, hipFuncAttributeMaxDynamicSharedMemorySize, dit::L_BYTES);
  dit::dit_kernel<<<256, 256, dit::L_BYTES, stream>>>((const u16*)d_in[0], (const u16*)d_in[1], (const u16*)d_in[2], (const u16*)d_in[3],
                                                      (const u16*)d_in[4], (const u16*)d_in[5], (const u16*)d_in[6], (u16*)d_out,
                                                      (char*)d_ws);
}
```

```cpp
#include <hip/hip_runtime.h>
#include <hip/hip_bf16.h>

typedef unsigned short u16;
typedef unsigned u32;
typedef unsigned long long u64;
typedef u32 v4u __attribute__((ext_vector_type(4)));
typedef u32 v2u __attribute__((ext_vector_type(2)));
typedef float f4 __attribute__((ext_vector_type(4)));
typedef float f2 __attribute__((ext_vector_type(2)));
typedef __bf16 b8 __attribute__((ext_vector_type(8)));
typedef __bf16 b2 __attribute__((ext_vector_type(2)));
typedef short s4 __attribute__((ext_vector_type(4)));
#define LAS __attribute__((address_space(3)))
#define GAS __attribute__((address_space(1)))
#define DEV __device__ __forceinline__
#define UNROLL _Pragma("unroll")
typedef __amdgpu_buffer_rsrc_t rsrc_t;

#define STAMP 0

namespace dit {

constexpr int SC1 = 16;
constexpr u32 SPIN_CAP = 1u << 16;
constexpr int OOB = 0x7ffffff0;

constexpr int WS_EPOCH = 0;
constexpr int WS_START = 1024;
constexpr int WS_RTF = 4096;
constexpr int WS_FLAG = 8192;
constexpr int WS_MOD = WS_FLAG + 256 * 512;
constexpr int WS_ML = WS_MOD + 2 * 2304 * 8;
constexpr int WS_ST = WS_ML + 256 * 512;
constexpr int WS_Q = WS_ST + 32 * 4096;
constexpr int WS_K = WS_Q + 64 * 24576;
constexpr int WS_V = WS_K + 64 * 24576;
constexpr int WS_OP = WS_V + 64 * 24576;
constexpr int WS_X1 = WS_OP + 256 * 24576;
constexpr int WS_HH = WS_X1 + 32 * 49152;
constexpr int WS_W1T = WS_HH + 32 * 196608;
constexpr int WS_W2T = WS_W1T + 64 * 39 * 1024;
constexpr int WS_END = WS_W2T + 16 * 72 * 1024;
constexpr int WS_STAMP = WS_END;
static_assert(WS_Q % 128 == 0 && WS_MOD % 128 == 0 && WS_ML % 128 == 0, "line alignment");

constexpr int L_WR = 0;
constexpr int L_Q = 0;
constexpr int L_P = 24576;
constexpr int L_MX = 57344;
constexpr int L_OC = 58368;
constexpr int L_GATE = 70656;
constexpr int L_STL = 73728;
constexpr int L_RED = 0;
constexpr int L_ACT = 110592;
constexpr int L_SC = L_ACT;
constexpr int L_MODA = 160768;
constexpr int L_BYTES = 163840;
#define K_W1P 16
#define K_W1B0 24
#define K_W1B 28
#define K_W1C0 34
#define K_W2C 1
#define K_W2W0 9
#define K_W2W 15
#define K_W2D0 21
#define K_W2D 57
#define K_W2E0 65
#define K_W2E 69
#define K_HDELAY 0
#define K_TOUCH 1
constexpr int W1_P = K_W1P, W1_B0 = K_W1B0, W1_B = K_W1B, W1_C0 = K_W1C0;
constexpr int W2_C = K_W2C, W2_W0 = K_W2W0, W2_W = K_W2W, W2_D0 = K_W2D0, W2_D = K_W2D, W2_E0 = K_W2E0, W2_E = K_W2E;
DEV constexpr bool w1_in_v(int n) { return n < 23; }
DEV constexpr bool w2_in_v(int n) { return n >= 48 && n < 58; }

#define ZERO4 (f4{0.f, 0.f, 0.f, 0.f})
#define FENCE() __builtin_amdgcn_sched_barrier(0)
DEV f4 mfma(v4u a, v4u b, f4 c) {
  return __builtin_amdgcn_mfma_f32_16x16x32_bf16(__builtin_bit_cast(b8, a), __builtin_bit_cast(b8, b), c, 0, 0, 0);
}
template <bool SWAP> DEV void mm(f4& c, const v4u& w, const v4u& x, bool w_in_v, bool first) {
  if (first) {
    if (w_in_v) { if (SWAP) asm volatile("v_mfma_f32_16x16x32_bf16 %0, %2, %1, 0" : "=&v"(c) : "v"(w), "v"(x));
                  else asm volatile("v_mfma_f32_16x16x32_bf16 %0, %1, %2, 0" : "=&v"(c) : "v"(w), "v"(x)); }
    else { if (SWAP) asm volatile("v_mfma_f32_16x16x32_bf16 %0, %2, %1, 0" : "=&v"(c) : "a"(w), "v"(x));
           else asm volatile("v_mfma_f32_16x16x32_bf16 %0, %1, %2, 0" : "=&v"(c) : "a"(w), "v"(x)); }
  } else {
    if (w_in_v) { if (SWAP) asm volatile("v_mfma_f32_16x16x32_bf16 %0, %2, %1, %0" : "+v"(c) : "v"(w), "v"(x));
                  else asm volatile("v_mfma_f32_16x16x32_bf16 %0, %1, %2, %0" : "+v"(c) : "v"(w), "v"(x)); }
    else { if (SWAP) asm volatile("v_mfma_f32_16x16x32_bf16 %0, %2, %1, %0" : "+v"(c) : "a"(w), "v"(x));
           else asm volatile("v_mfma_f32_16x16x32_bf16 %0, %1, %2, %0" : "+v"(c) : "a"(w), "v"(x)); }
  }
}
template <int I> struct IC { static constexpr int v = I; };
template <int I, int N, class F> DEV void sfor(F&& f) {
  if constexpr (I < N) { f(IC<I>{}); sfor<I + 1, N>(f); }
}
#define NOP8() do { FENCE(); asm volatile("s_nop 7"); FENCE(); } while (0)
DEV float asf(u32 v) { return __builtin_bit_cast(float, v); }
DEV u32 asu(float v) { return __builtin_bit_cast(u32, v); }
DEV u32 pk(float a, float b) { f2 v = {a, b}; return __builtin_bit_cast(u32, __builtin_convertvector(v, b2)); }
DEV float lo(u32 v) { return __builtin_bit_cast(float, v << 16); }
DEV float hi(u32 v) { return __builtin_bit_cast(float, v & 0xffff0000u); }
DEV float rbf(float v) { return lo(pk(v, v)); }
DEV float mx2(float a, float b) { return __builtin_elementwise_maximum(a, b); }
DEV float mx4(f4 a) { return mx2(mx2(mx2(a[0], a[1]), a[2]), a[3]); }
DEV void opq(float& a) { asm volatile("" : "+v"(a)); }
DEV void opq(f2& a) { asm volatile("" : "+v"(a)); }
DEV float v_fma(float a, float b, float c) { float d; asm volatile("v_fma_f32 %0, %1, %2, %3" : "=v"(d) : "v"(a), "v"(b), "v"(c)); return d; }
DEV float v_mul(float a, float b) { float d; asm volatile("v_mul_f32 %0, %1, %2" : "=v"(d) : "v"(a), "v"(b)); return d; }
DEV float v_inc(float a) { float d; asm volatile("v_add_f32 %0, 1.0, %1" : "=v"(d) : "v"(a)); return d; }
DEV float v_exp(float a) { float d; asm volatile("v_exp_f32 %0, %1" : "=v"(d) : "v"(a)); return d; }
DEV float v_rcp(float a) { float d; asm volatile("v_rcp_f32 %0, %1" : "=v"(d) : "v"(a)); return d; }
DEV u32 v_pk(float a, float b) { u32 d; asm volatile("v_cvt_pk_bf16_f32 %0, %1, %2" : "=v"(d) : "v"(a), "v"(b)); return d; }
DEV int vopq(int a) { asm volatile("" : "+v"(a)); return a; }
DEV int sopq(int a) { asm volatile("" : "+s"(a)); return a; }
DEV int mod3(int a) { return a >= 3 ? a - 3 : a; }
DEV rsrc_t mkrs(const void* p, int bytes) { return __builtin_amdgcn_make_buffer_rsrc((void*)p, 0, bytes, 0x00020000); }
template <int AUX> DEV v4u ld16(rsrc_t r, int v, int s) { return __builtin_amdgcn_raw_buffer_load_b128(r, v, s, AUX); }
template <int AUX> DEV v2u ld8(rsrc_t r, int v, int s) { return __builtin_amdgcn_raw_buffer_load_b64(r, v, s, AUX); }
template <int AUX> DEV void st16(v4u x, rsrc_t r, int v) { __builtin_amdgcn_raw_buffer_store_b128(x, r, v, 0, AUX); }
template <int AUX> DEV void st8(v2u x, rsrc_t r, int v) { __builtin_amdgcn_raw_buffer_store_b64(x, r, v, 0, AUX); }
#define LP(T, a) ((LAS T*)(size_t)(u32)(a))
DEV v4u lds16(int p) { return *LP(v4u, p); }
DEV f4 ldsf4(int p) { return *LP(f4, p); }
DEV void sts16(int p, v4u x) { *LP(v4u, p) = x; }
DEV v4u trfrag(int p, int rs4) {
  s4 a = __builtin_amdgcn_ds_read_tr16_b64_v4i16(LP(s4, p));
  s4 b = __builtin_amdgcn_ds_read_tr16_b64_v4i16(LP(s4, p + rs4));
  v2u x = __builtin_bit_cast(v2u, a), y = __builtin_bit_cast(v2u, b);
  return v4u{x.x, x.y, y.x, y.y};
}
DEV int fsw(int r) { return (r & 2) | ((r >> 1) & 4); }
DEV void bar() {
  asm volatile("s_waitcnt lgkmcnt(0)" ::: "memory");
  __builtin_amdgcn_s_barrier();
  asm volatile("" ::: "memory");
  FENCE();
}
template <int N> DEV void vmwait() { asm volatile("s_waitcnt vmcnt(%0)" :: "i"(N) : "memory"); }
DEV void dma3(u32 lds, int v0, int v1, int v2, rsrc_t r, int soff) {
  asm volatile("s_mov_b32 m0, %0\n\ts_nop 0\n\tbuffer_load_dwordx4 %1, %4, %5 offen lds\n\t"
               "buffer_load_dwordx4 %2, %4, %5 offen offset:1024 lds\n\tbuffer_load_dwordx4 %3, %4, %5 offen offset:2048 lds"
               :: "s"(lds), "v"(v0), "v"(v1), "v"(v2), "s"(r), "s"(soff) : "memory");
}
DEV void touch(int lds, rsrc_t rw, int voff) {
  asm volatile("s_mov_b32 m0, %0\n\ts_nop 0\n\tbuffer_load_dword %1, %2, 0 offen sc1 lds" :: "s"(lds), "v"(voff), "s"(rw) : "memory");
}
DEV bool same(v2u v, v2u tag) { return __all(v.x == tag.x && v.y == tag.y); }
DEV void poll_from(v2u v, rsrc_t rw, int voff, v2u tag) {
  if (!same(v, tag)) {
#pragma unroll 1
    for (u32 s = 0; s < SPIN_CAP; ++s) {
      asm volatile("" ::: "memory");
      if (same(ld8<SC1>(rw, voff, 0), tag)) break;
    }
  }
  asm volatile("" ::: "memory");
}
DEV void poll(rsrc_t rw, int voff, v2u tag) { poll_from(ld8<SC1>(rw, voff, 0), rw, voff, tag); }
template <int CTRL> DEV float dpp(float v) {
  return __builtin_bit_cast(float, __builtin_amdgcn_update_dpp(0, __builtin_bit_cast(int, v), CTRL, 0xf, 0xf, true));
}
DEV float xg_sum(float v) {
  auto a = __builtin_amdgcn_permlane16_swap(asu(v), asu(v), false, false);
  v = asf(a[0]) + asf(a[1]);
  auto b = __builtin_amdgcn_permlane32_swap(asu(v), asu(v), false, false);
  return asf(b[0]) + asf(b[1]);
}
DEV v4u pair_swap(v2u p0, v2u p1) {
  auto a = __builtin_amdgcn_permlane16_swap(p0.x, p1.x, false, false);
  auto b = __builtin_amdgcn_permlane16_swap(p0.y, p1.y, false, false);
  return v4u{a[0], b[0], a[1], b[1]};
}
DEV v2u pk4(f4 a) { return v2u{pk(a[0], a[1]), pk(a[2], a[3])}; }

DEV void mod_request(v2u (&v)[3], rsrc_t rw, int voff) {
  UNROLL for (int i = 0; i < 3; ++i) v[i] = ld8<SC1>(rw, voff, i * 2048);
}
template <bool ONE_B>
DEV void mod_finish(v2u (&v)[3], rsrc_t rw, int voff, int d0, int d1, int d2, v2u gtag, int tid, float mul) {
  auto ok = [&]() {
    return __all((((v[0].y ^ gtag.y) | (v[1].y ^ gtag.y) | (v[2].y ^ gtag.y)) |
                  (((v[0].x ^ gtag.x) | (v[1].x ^ gtag.x) | (v[2].x ^ gtag.x)) >> 16)) == 0);
  };
  if (!ok()) {
#pragma unroll 1
    for (u32 s = 0; s < SPIN_CAP; ++s) {
      asm volatile("" ::: "memory");
      mod_request(v, rw, voff);
      if (ok()) break;
    }
  }
  float f0 = lo(v[0].x), f1 = lo(v[1].x), f2_ = lo(v[2].x);
  if (ONE_B) { f1 = tid >= 128 ? rbf(1.f + f1) : f1; f2_ = rbf(1.f + f2_); }
  *LP(float, d0) = f0 * mul; *LP(float, d1) = f1 * mul; *LP(float, d2) = f2_ * mul;
}

#define TS_DECL()
#define TS(i) do {} while (0)

constexpr int RING = 4;
#define FRAG(n, so) ld16<SC1>(rw, l16[((n) % 12) / 4] + ((n) % 4) * 1024, (so)[(n) / 12])
#define FRESH_L16() UNROLL for (int j_ = 0; j_ < 3; ++j_) l16[j_] = vopq(l16[j_])

template <bool VTYPE, class F, class G>
DEV void phaseA_gemm(v4u (&wf)[12][3], const int (&ha)[12], rsrc_t rw, const int (&so)[6], F wodma, G wtr) {
  auto hfrag = [&](int n) { return lds16(ha[(n % 24) >> 1] + ((n / 24) * 2 + (n & 1)) * 256); };
  f4 acc[2][2][3];
  auto epi = [&](int tp, int ft) {
    const v2u a = pk4(acc[tp][0][ft]), b = pk4(acc[tp][1][ft]);
    st16<SC1>(VTYPE ? v4u{a.x, a.y, b.x, b.y} : pair_swap(a, b), rw, so[tp * 3 + ft]);
  };
  v4u ring[RING];
  UNROLL for (int n = 0; n < RING; ++n) ring[n] = hfrag(n);
  FENCE();
  sfor<0, 48>([&](auto ic) {
    constexpr int n = ic.v, tp = n / 24, s = (n % 24) >> 1, t = n & 1;
    const v4u hb = ring[n % RING];
    mm<VTYPE>(acc[tp][t][0], wf[s][0], hb, s >= 9, s == 0);
    FENCE();
    if (n + RING < 48) ring[n % RING] = hfrag(n + RING);
    FENCE();
    mm<VTYPE>(acc[tp][t][1], wf[s][1], hb, s >= 9, s == 0);
    FENCE();
    if constexpr (n < 9) wf[9 + n / 3][n % 3] = wtr(9 + n / 3, n % 3);
    if (n == 27 || n == 31 || n == 35) epi(0, (n - 27) / 4);
    FENCE();
    mm<VTYPE>(acc[tp][t][2], wf[s][2], hb, s >= 9, s == 0);
    FENCE();
    if (n == 24) __builtin_amdgcn_s_barrier();
    if (n % 5 == 3 && n < 45) wodma(n / 5);
    FENCE();
  });
  NOP8();
  UNROLL for (int ft = 0; ft < 3; ++ft) epi(1, ft);
}

__global__ void __launch_bounds__(256, 1)
dit_kernel(const u16* __restrict__ x, const u16* __restrict__ cvec, const u16* __restrict__ wqkv, const u16* __restrict__ wo,
           const u16* __restrict__ wmod, const u16* __restrict__ w1, const u16* __restrict__ w2, u16* __restrict__ out, char* ws) {
  extern __shared__ __attribute__((aligned(16))) char smem[];
  const int lb = sopq((int)(u32)(size_t)(LAS char*)smem);
  const int tid = threadIdx.x, lane = tid & 63, w = __builtin_amdgcn_readfirstlane(tid >> 6);
  const int i16 = lane & 15, g = lane >> 4;
  const int xcd = blockIdx.x & 7, idx = blockIdx.x >> 3;
  const int team = xcd * 4 + (idx >> 3), mem = idx & 7, bat = team >> 4, me = team * 8 + mem, tq = team & 3;
  TS_DECL();
  TS(0);
  {
    u64 pc; u32 len;
    asm volatile("s_getpc_b64 %0\ndit_code_begin:\n\ts_mov_b32 %1, dit_code_end - dit_code_begin" : "=s"(pc), "=s"(len));
    const rsrc_t rc = mkrs((const void*)(pc & ~127ull), len);
    const u32 per = (len / 32 + 127) & ~127u;
    if (w == 0) {
      const int off = lane * 128 < (int)per ? idx * per + lane * 128 : OOB;
      asm volatile("s_mov_b32 m0, %0\n\ts_nop 0\n\tbuffer_load_dword %1, %2, 0 offen lds" :: "s"(lb + L_MODA), "v"(off), "s"(rc) : "memory");
    }
  }
  asm volatile("" :: "s"(x), "s"(cvec), "s"(wqkv), "s"(wo), "s"(wmod), "s"(w1), "s"(w2), "s"(out), "s"(ws));
  TS(28);
  const rsrc_t rw = mkrs(ws, WS_END);
  const rsrc_t rx = mkrs(x, 2048 * 384 * 2);
  v2u tag, gtag;
  static_assert(WS_EPOCH == 0 && WS_FLAG == 0x2000, "offsets in EPOCH_ASK");
#define EPOCH_ASK() u64 ep_, tch0_, tch1_, tch2_, tch3_; \
  asm volatile("s_load_dwordx2 %0, %5, 0x0\n\ts_load_dwordx2 %1, %5, %6 offset:0x2000\n\ts_load_dwordx2 %2, %5, %6 offset:0x2080\n\t" \
               "s_load_dwordx2 %3, %5, %6 offset:0x2100\n\ts_load_dwordx2 %4, %5, %6 offset:0x2180" \
               : "=&s"(ep_), "=&s"(tch0_), "=&s"(tch1_), "=&s"(tch2_), "=&s"(tch3_) : "s"(ws), "s"(me * 512) : "memory")
#define EPOCH_WAIT() asm volatile("s_waitcnt lgkmcnt(0)" : "+s"(ep_), "+s"(tch0_), "+s"(tch1_), "+s"(tch2_), "+s"(tch3_) :: "memory"); \
  ep_ += 1; tag = v2u{(u32)ep_, (u32)(ep_ >> 32)}; gtag = v2u{(u32)ep_ << 16, (u32)(ep_ >> 16)}
  const int c_tt = mem >> 1, c_ch = mem & 1;
  const int r3 = xcd % 3;
  v4u wof[36];
  int pub, tra[3][3], woo[3];
  const rsrc_t rwo = mkrs(wo, 384 * 384 * 2);
  int srow3[3], scol3[3];
  UNROLL for (int i = 0; i < 3; ++i) {
    const int q = i * 64 + lane, r = (q * 171) >> 12;
    srow3[i] = 8 * w + r;
    scol3[i] = ((q - r * 24) ^ ((r & 2) | ((w & 1) << 2))) * 16 - i * 1024;
  }
  auto stage_off = [&](int (&o)[3], int stride) { UNROLL for (int i = 0; i < 3; ++i) o[i] = vopq(srow3[i] * stride + scol3[i]); };
  auto tr_setup = [&]() {
    const int row = 8 * g + (i16 >> 2), t0 = lb + row * 384 + 8 * (lane & 1), f = fsw(row) ^ ((lane & 3) >> 1);
    UNROLL for (int ft = 0; ft < 3; ++ft) {
      const int t = t0 + ((6 * w + 2 * ft) ^ f) * 16;
      tra[0][ft] = vopq(t + L_ACT); tra[1][ft] = vopq(t + L_WR); tra[2][ft] = vopq(t + L_WR + 5 * 12288);
    }
  };
  auto wodma = [&](int s) {
    dma3(lb + (s < 9 ? L_WR + s * 12288 : L_ACT + (s - 9) * 12288) + w * 3072, woo[0], woo[1], woo[2], rwo,
         (mod3(s / 4 + r3) * 4 + (s & 3)) * 24576 + c_ch * 384);
  };

  if (mem < 6) {
    const rsrc_t rq = mkrs(wqkv, 384 * 1152 * 2);
    const int hs = (mem >> 1) & 1;
    EPOCH_ASK();
    v4u xr[12];
    const int xo = (g * 384 + i16 * 8) * 2;
    auto xload = [&](int it) {
      const int so = (team * 64 + ((it >> 1) ^ hs) * 32 + w * 8 + ((it ^ mem) & 1) * 4) * 768;
      asm volatile("buffer_load_dwordx4 %0, %3, %4, %5 offen\n\tbuffer_load_dwordx4 %1, %3, %4, %5 offen offset:256\n\t"
                   "buffer_load_dwordx4 %2, %3, %4, %5 offen offset:512"
                   : "=&v"(xr[3 * it]), "=&v"(xr[3 * it + 1]), "=&v"(xr[3 * it + 2]) : "v"(xo), "s"(rx), "s"(so) : "memory");
    };
    xload(0); xload(1);
    asm volatile("s_mov_b32 m0, %0\n\ts_nop 0\n\tbuffer_load_dword %1, %2, 0 offen lds"
                 :: "s"(lb + L_MODA + 256 + w * 256), "v"(lane < 9 ? (((team * 6 + mem) * 4 + w) * 9 + lane) * 128 : OOB), "s"(rq) : "memory");
    if (xcd < 2) {
      const rsrc_t rm = mkrs(wmod, 384 * 2304 * 2);
      const int n = ((tq * 6 + mem) * 4 + w) * 11 + lane;
      asm volatile("s_mov_b32 m0, %0\n\ts_nop 0\n\tbuffer_load_dword %1, %2, 0 offen lds"
                   :: "s"(lb + L_MODA + 1280 + w * 256), "v"(lane < 11 && n < (xcd ? 512 : 1024) ? (256 + (n & 127)) * 4608 + (xcd * 8 + (n >> 7)) * 128 : OOB),
                      "s"(rm) : "memory");
    }
    xload(2); xload(3);
    int woff[3];
    stage_off(woff, 2304);
    const int ks0 = team % 12, wm0 = sopq(lb + w * 3072);
    int ko = ks0 * 4096, wso = ks0 * 73728 + mem * 384;
    const int hb0 = lb + L_ACT + g * 1024 + ((i16 ^ (((ks0 & 1) << 2) | g)) * 16), hb1 = hb0 ^ 64;
    int ha[12];
    auto wdma = [&](int s) {
      ha[s] = vopq(((s & 1) ? hb1 : hb0) + ko);
      asm volatile("s_add_u32 m0, %2, %3\n\ts_add_u32 %0, %0, 0x1000\n\tbuffer_load_dwordx4 %4, %7, %1 offen lds\n\t"
                   "buffer_load_dwordx4 %5, %7, %1 offen offset:1024 lds\n\tbuffer_load_dwordx4 %6, %7, %1 offen offset:2048 lds\n\t"
                   "s_add_u32 %1, %1, 0x12000\n\ts_cmp_eq_u32 %0, 0xc000\n\ts_cselect_b32 %0, 0, %0\n\ts_cselect_b32 %1, %8, %1"
                   : "+s"(ko), "+s"(wso)
                   : "s"(wm0), "i"(s < 3 ? L_ACT + s * 12288 : L_WR + (s - 3) * 12288), "v"(woff[0]), "v"(woff[1]), "v"(woff[2]), "s"(rq), "s"(mem * 384)
                   : "memory", "scc");
      FENCE();
    };
    wdma(0); wdma(1); wdma(2);
    const int mo = vopq(WS_MOD + bat * 2304 * 8 + tid * 8);
    asm volatile("s_waitcnt vmcnt(9)"
                 : "+v"(xr[0]), "+v"(xr[1]), "+v"(xr[2]), "+v"(xr[3]), "+v"(xr[4]), "+v"(xr[5]), "+v"(xr[6]), "+v"(xr[7]), "+v"(xr[8]),
                   "+v"(xr[9]), "+v"(xr[10]), "+v"(xr[11]) :: "memory");
    EPOCH_WAIT();
    FENCE();
    TS(25);
    f2 xn[4][12];
    float st[8];
    UNROLL for (int it = 0; it < 4; ++it) {
      wdma(3 + it);
      f2 s = {0.f, 0.f}, q = {0.f, 0.f};
      UNROLL for (int e = 0; e < 12; ++e) {
        xn[it][e] = f2{lo(xr[3 * it + e / 4][e & 3]), hi(xr[3 * it + e / 4][e & 3])};
        s += xn[it][e]; q += xn[it][e] * xn[it][e];
      }
      st[it] = s[0] + s[1]; st[4 + it] = q[0] + q[1];
      FENCE();
    }
    v2u mv[3];
    asm volatile("buffer_load_dwordx2 %0, %3, %4, 0 offen sc1\n\tbuffer_load_dwordx2 %1, %3, %4, 0 offen offset:2048 sc1\n\t"
                 "buffer_load_dwordx2 %2, %3, %4, %5 offen sc1"
                 : "=&v"(mv[0]), "=&v"(mv[1]), "=&v"(mv[2]) : "v"(mo), "s"(rw), "s"(4096) : "memory");
    tr_setup();
    auto wtr = [&](int s, int ft) {
      return trfrag(s < 3 ? tra[0][ft] + s * 12288 : s < 8 ? tra[1][ft] + (s - 3) * 12288 : tra[2][ft] + (s - 8) * 12288, 4 * 384);
    };
    v4u wf[12][3];
    vmwait<15>();
    bar();
    wdma(7);
    UNROLL for (int n = 0; n < 9; ++n) wf[n / 3][n % 3] = wtr(n / 3, n % 3);
    UNROLL for (int j = 0; j < 8; ++j) st[j] += dpp<0x128>(st[j]);
    UNROLL for (int j = 0; j < 8; ++j) st[j] += dpp<0x124>(st[j]);
    UNROLL for (int j = 0; j < 8; ++j) st[j] += dpp<0x122>(st[j]);
    UNROLL for (int j = 0; j < 8; ++j) st[j] += dpp<0x121>(st[j]);
    UNROLL for (int it = 0; it < 4; ++it) {
      const float mu = st[it] * (1.f / 384.f);
      st[4 + it] = __builtin_amdgcn_rsqf(fmaxf(st[4 + it] * (1.f / 384.f) - mu * mu, 0.f) + 1e-5f);
      st[it] = -mu * st[4 + it];
    }
    FENCE();
    wdma(8);
    UNROLL for (int it = 0; it < 4; ++it)
      UNROLL for (int e = 0; e < 12; ++e) { xn[it][e] = xn[it][e] * st[4 + it] + st[it]; opq(xn[it][e]); }
    const int mb = vopq(lb + L_MODA + i16 * 16);
    const int md = vopq(lb + L_MODA + (tid >> 7) * 512 + ((tid >> 2) & 1) * 256 + ((tid >> 3) & 15) * 16 + (tid & 3) * 4);
    FENCE();
    TS(26);
    asm volatile("s_waitcnt vmcnt(6)" : "+v"(mv[0]), "+v"(mv[1]), "+v"(mv[2]) :: "memory");
    mod_finish<true>(mv, rw, mo, md, md + 1024, md + 2048, gtag, tid, mem < 2 ? 0.07216878364870322f * 1.4426950408889634f : 1.f);
    int hw[2];
    UNROLL for (int j = 0; j < 2; ++j) hw[j] = vopq(lb + L_ACT + i16 * 1024 + (((w * 8 + ((j ^ mem) & 1) * 4 + g) ^ (i16 & 7)) * 16));
    bar();
    TS(2);
    f4 tg[3][4];
    UNROLL for (int i = 0; i < 3; ++i) {
      tg[i][0] = ldsf4(mb + i * 512); tg[i][1] = ldsf4(mb + i * 512 + 256);
      tg[i][2] = ldsf4(mb + i * 512 + 1536); tg[i][3] = ldsf4(mb + i * 512 + 1792);
    }
    FENCE();
    UNROLL for (int i = 0; i < 3; ++i) {
      wdma(9 + i);
      const f2 tt[4] = {{tg[i][0][0], tg[i][0][1]}, {tg[i][0][2], tg[i][0][3]}, {tg[i][1][0], tg[i][1][1]}, {tg[i][1][2], tg[i][1][3]}};
      const f2 gg[4] = {{tg[i][2][0], tg[i][2][1]}, {tg[i][2][2], tg[i][2][3]}, {tg[i][3][0], tg[i][3][1]}, {tg[i][3][2], tg[i][3][3]}};
      UNROLL for (int it = 0; it < 4; ++it) {
        v4u h;
        UNROLL for (int e = 0; e < 4; ++e) { const f2 v = xn[it][i * 4 + e] * gg[e] + tt[e]; h[e] = pk(v[0], v[1]); }
        sts16(hw[it & 1] + i * 16384 + (it >> 1) * 512, h);
      }
      FENCE();
    }
    vmwait<9>();
    bar();
    TS(1);
    UNROLL for (int n = 9; n < 27; ++n) wf[n / 3][n % 3] = wtr(n / 3, n % 3);
    FENCE();
    int so[6];
    const int hd = mem & 1;
    {
      const bool vt = mem >= 4;
      const int base = (vt ? WS_V + 3 * w * 1024 : (mem < 2 ? WS_Q : WS_K) + 6 * w * 1024) + (team * 2 + hd) * 24576;
      const int lo_ = vt ? lane * 16 : (g >> 1) * 1024 + ((g & 1) * 16 + i16) * 16;
      UNROLL for (int j = 0; j < 6; ++j) so[j] = vopq(lo_ + sopq(base + ((j / 3) ^ hs) * (vt ? 12288 : 512) + (j % 3) * (vt ? 1024 : 2048)));
      const int cb = WS_FLAG + (bat * 4 + hd * 2) * 32 * 512;
      const int cq = cb + (team & 15) * 1024 + (lane >> 1) * 16384 + (lane & 1) * 512;
      const int ck = cb + ((xcd & 3) >> 1) * 16384 + (xcd & 1) * 512 + ((mem < 4 ? 1 : 5) + tq) * 8 + lane * 1024;
      pub = vopq(lane < (mem < 2 ? 4 : 16) ? (mem < 2 ? cq : ck) : lane == 63 ? WS_START + (int)blockIdx.x * 8 : OOB);
    }
    stage_off(woo, 768);
    FENCE();
    vmwait<0>();
    bar();
    TS(3);
    if (mem >= 4) phaseA_gemm<true>(wf, ha, rw, so, wodma, wtr);
    else phaseA_gemm<false>(wf, ha, rw, so, wodma, wtr);
    TS(4);
    bar();
    wodma(9); wodma(10); wodma(11);
    FENCE();
  } else {
    const int hidx = team * 2 + (mem - 6);
    const int u2 = hidx < 12 ? hidx : hidx - 24;
    const int w2off = (u2 / 6) * 384 * 768 + (u2 % 6) * 128;
    const int yb = hidx >= 36, yu = hidx - 12 - yb * 24;
    const int srow = lane >> 3, sc = (lane & 7) ^ fsw(srow + 8 * w);
    const int trr = 8 * g + (i16 >> 2);
    const int trh = vopq(lb + L_WR + trr * 128 + ((2 * w + ((lane & 3) >> 1)) ^ fsw(trr)) * 16 + 8 * (lane & 1));
    auto unit_dma = [&](int buf, rsrc_t r, int stride, int soff) {
      const int v = (8 * w + srow) * stride + sc * 16;
      UNROLL for (int s = 0; s < 12; ++s)
        asm volatile("s_mov_b32 m0, %0\n\ts_nop 0\n\tbuffer_load_dwordx4 %1, %2, %3 offen lds"
                     :: "s"(lb + L_WR + buf * 49152 + s * 4096 + w * 1024), "v"(v), "s"(r), "s"(soff + s * 32 * stride) : "memory");
    };
    u32 cv[3];
    {
      const rsrc_t rc = mkrs(cvec, 768 * 2);
      const rsrc_t rX = mkrs(hidx < 36 ? wmod : w2, sopq(__builtin_amdgcn_readfirstlane(hidx < 36 ? 384 * 2304 * 2 : hidx < 48 ? 384 * 1536 * 2 : 0)));
      const rsrc_t rY = mkrs(w1, sopq(__builtin_amdgcn_readfirstlane(hidx >= 12 && hidx < 60 ? 384 * 1536 * 2 : 0)));
      if (K_HDELAY && hidx >= 12) __builtin_amdgcn_s_sleep(K_HDELAY);
      EPOCH_ASK();
      asm volatile("buffer_load_ushort %0, %3, %4, 0 offen\n\tbuffer_load_ushort %1, %3, %4, 0 offen offset:512\n\t"
                   "buffer_load_ushort %2, %3, %4, 0 offen offset:1024"
                   : "=&v"(cv[0]), "=&v"(cv[1]), "=&v"(cv[2]) : "v"(tid * 2), "s"(rc) : "memory");
      unit_dma(0, rX, hidx < 36 ? 4608 : 768, hidx < 36 ? hidx * 128 : w2off);
      unit_dma(1, rY, 3072, yu * 128);
      asm volatile("s_waitcnt vmcnt(24)" : "+v"(cv[0]), "+v"(cv[1]), "+v"(cv[2]) :: "memory");
      EPOCH_WAIT();
    }
    UNROLL for (int i = 0; i < 3; ++i) {
      const float c = lo(cv[i]);
      *LP(u16, lb + L_SC + (tid + 256 * i) * 2) = (u16)pk(c * __builtin_amdgcn_rcpf(1.f + __expf(-c)), 0.f);
    }
    auto w2_store = [&](const v4u (&f)[12]) {
      const int ft = 4 * (u2 % 6) + w;
      const int dst = lane * 16 + WS_W2T + (((ft / 6) * 4 + u2 / 6) * 72 + ft % 6) * 1024;
      UNROLL for (int s = 0; s < 12; ++s) st16<SC1>(f[s], rw, dst + s * 6144);
    };
    const int ch = hidx / 6, pos = ch == 2 ? 4 : ch == 3 ? 2 : ch == 4 ? 3 : ch;
    const int go0 = vopq(g ? OOB : WS_MOD + (pos * 384 + (hidx % 6) * 64 + w * 16 + lane) * 8), go1 = vopq(g ? OOB : go0 + 2304 * 8);
    const int sca = vopq(lb + L_SC + (i16 & 1) * 768 + g * 16);
    FENCE();
    TS(25);
    vmwait<12>();
    bar();
    TS(26);
    if (hidx < 48) {
      v4u fb[12];
      UNROLL for (int s = 0; s < 12; ++s) fb[s] = trfrag(trh + s * 4096, 4 * 128);
      if (hidx < 36) {
        v4u fa[12];
        UNROLL for (int s = 0; s < 12; ++s) fa[s] = lds16(sca + s * 64);
        FENCE();
        f4 acc = ZERO4;
        UNROLL for (int s = 0; s < 12; ++s) acc = mfma(fa[s], fb[s], acc);
        const u32 p = pk(acc[0], acc[1]);
        st8<SC1>(v2u{gtag.x | (p & 0xffffu), gtag.y}, rw, go0);
        st8<SC1>(v2u{gtag.x | (p >> 16), gtag.y}, rw, go1);
        FENCE();
        TS(29);
        if (hidx < 12) unit_dma(1, mkrs(w2, 384 * 1536 * 2), 768, w2off);
      } else {
        w2_store(fb);
      }
    }
    TS(1);
    tr_setup();
    stage_off(woo, 768);
    vmwait<0>();
    bar();
    if (hidx < 60) {
      v2u mv[3];
      const int mo = vopq(WS_MOD + (yb * 2304 + 768) * 8 + tid * 8);
      v4u f[12];
      UNROLL for (int s = 0; s < 12; ++s) f[s] = trfrag(trh + 49152 + s * 4096, 4 * 128);
      if (hidx < 12) {
        w2_store(f);
        bar();
      } else {
        mod_request(mv, rw, mo);
        f2 wv[12][4];
        UNROLL for (int s = 0; s < 12; ++s)
          UNROLL for (int e = 0; e < 4; ++e) { wv[s][e] = f2{lo(f[s][e]), hi(f[s][e])}; opq(wv[s][e]); }
        mod_finish<true>(mv, rw, mo, lb + L_MODA + tid * 4, lb + L_MODA + tid * 4 + 1024, lb + L_MODA + tid * 4 + 2048, gtag, tid, 1.f);
        bar();
        TS(3);
        const int ft = 4 * yu + w;
        const int fdst = WS_W1T + ((yb * 32 + ft / 3) * 39 + ft % 3) * 1024 + lane * 16;
        const int gb = vopq(lb + L_MODA + g * 32);
        f4 gq[12][2], tq4[12][2];
        UNROLL for (int s = 0; s < 12; ++s) { gq[s][0] = ldsf4(gb + 1536 + s * 128); gq[s][1] = ldsf4(gb + 1552 + s * 128); }
        FENCE();
        v4u sc4[12];
        UNROLL for (int s = 0; s < 12; ++s) {
          UNROLL for (int e = 0; e < 4; ++e) {
            const f2 v = wv[s][e] * f2{gq[s][e >> 1][(e & 1) * 2], gq[s][e >> 1][(e & 1) * 2 + 1]};
            sc4[s][e] = pk(v[0], v[1]);
          }
          st16<SC1>(sc4[s], rw, fdst + s * 3072);
          tq4[s][0] = ldsf4(gb + s * 128); tq4[s][1] = ldsf4(gb + 16 + s * 128);
          FENCE();
        }
        const v4u ones = {0x3f803f80u, 0x3f803f80u, 0x3f803f80u, 0x3f803f80u};
        f4 au = ZERO4, av = ZERO4;
        UNROLL for (int s = 0; s < 12; ++s) {
          au = mfma(ones, sc4[s], au);
          av = mfma(v4u{pk(tq4[s][0][0], tq4[s][0][1]), pk(tq4[s][0][2], tq4[s][0][3]), pk(tq4[s][1][0], tq4[s][1][1]),
                        pk(tq4[s][1][2], tq4[s][1][3])}, f[s], av);
        }
        const float u = au[0], v = av[0], uh = rbf(u), vh = rbf(v);
        v4u e = {pk(uh, uh), pk(u - uh, vh), pk(vh, v - vh), 0u};
        if (g) e = v4u{0u, 0u, 0u, 0u};
        st16<SC1>(e, rw, fdst + 12 * 3072);
      }
    }
    FENCE();
    TS(2);
    UNROLL for (int s = 0; s < 12; ++s) wodma(s);
    pub = lane == 0 ? WS_START + (int)blockIdx.x * 8
        : lane == 1 && hidx >= 12 && hidx < 60 ? WS_RTF + (8 + hidx - 12) * 8
        : lane == 2 && (hidx < 12 || (hidx >= 36 && hidx < 48)) ? WS_RTF + (64 + u2) * 8 : OOB;
    TS(4);
    __builtin_amdgcn_s_waitcnt(0xC07F);
    FENCE();
  }

  v4u w1f[39];
  const int fl0 = vopq(WS_FLAG + me * 512 + (lane < 9 ? lane : 8) * 8);
  FENCE();
  vmwait<9>();
  bar();
  v2u fv;
  if (w == 0) { st8<SC1>(tag, rw, pub); fv = ld8<SC1>(rw, fl0, 0); }
  TS(5);
  auto wotr = [&](int s, int ft) {
    return trfrag(s < 5 ? tra[1][ft] + s * 12288 : s < 9 ? tra[2][ft] + (s - 5) * 12288 : tra[0][ft] + (s - 9) * 12288, 4 * 384);
  };
  UNROLL for (int n = 0; n < 27; ++n) wof[n] = wotr(n / 3, n % 3);
  FENCE();
  int w1s[4], l16[3];
  UNROLL for (int j = 0; j < 4; ++j) w1s[j] = sopq(WS_W1T + (bat * 32 + mem * 4 + w) * 39936 + (j < 3 ? mod3(j + r3) : 3) * 12288);
  UNROLL for (int j = 0; j < 3; ++j) l16[j] = vopq(lane * 16 + j * 4096);
  const int rt_w1 = vopq(WS_RTF + (8 + bat * 24 + (lane < 24 ? lane : 23)) * 8);
  const int hd = (xcd >> 1) & 1, sp = (xcd & 1) * 2 + (idx & 1), qteam = bat * 16 + (idx >> 1), unit = qteam * 8 + hd * 4 + sp;
  const int fl = vopq(WS_FLAG + me * 512 + (lane < 8 ? lane : 7) * 8);
  const int fp = vopq(lane < 8 ? WS_FLAG + (team * 8 + lane) * 512 + mem * 8 : OOB);
  const int fpb = vopq(lane < 8 ? WS_FLAG + (qteam * 8 + lane) * 512 + 128 + (hd * 4 + sp) * 8 : OOB);
  int kso[3], vso[4], qa[3], pa[4], fo2[2], vv2[2];
  {
    const int kt0 = bat * 16 + sp * 4;
    UNROLL for (int j = 0; j < 3; ++j) {
      kso[j] = sopq(WS_K + ((kt0 + w) * 2 + hd) * 24576 + j * 8192);
      qa[j] = vopq(lb + L_Q + j * 8192 + g * 1024 + i16 * 16);
    }
    UNROLL for (int j = 0; j < 4; ++j) {
      vso[j] = sopq(WS_V + ((kt0 + j) * 2 + hd) * 24576 + 3 * w * 1024);
      pa[j] = vopq(lb + L_P + j * 8192 + lane * 16);
    }
    UNROLL for (int j = 0; j < 2; ++j) { fo2[j] = vopq(g * 1024 + i16 * 16 + j * 4096); vv2[j] = vopq(lane * 16 + j * 12288); }
  }
  const int t16 = vopq(tid * 16), qw = vopq(lb + L_Q + tid * 16);
  const int qso = sopq(WS_Q + (qteam * 2 + hd) * 24576);
  const int pst = (g >> 1) * 1024 + ((g & 1) * 16 + i16) * 16;
  const int mxa = vopq(lb + L_MX + i16 * 16), mxw = vopq(lb + L_MX + lane * 16 + w * 4), mxo = vopq(lb + L_MX + (w * 16 + i16) * 16);
  const int pw = vopq(lb + L_P + w * 8192 + lane * 16), lso = vopq(lb + L_P + w * 1024 + lane * 16);
  int oso[3];
  UNROLL for (int dt = 0; dt < 3; ++dt) oso[dt] = vopq(WS_OP + unit * 24576 + (6 * w + 2 * dt) * 1024 + pst);
  const int mlo = vopq(g ? OOB : WS_ML + unit * 512 + w * 128 + i16 * 8);
  const int rt_w2 = vopq(WS_RTF + (64 + (lane < 24 ? lane : 23)) * 8);
  FENCE();
  if (w == 0) poll_from(fv, rw, fl0, tag);
  vmwait<0>();
  bar();
  TS(7);

  const int e_th = mem & 1, e_cs = mem >> 1;
  v4u w2f[72];
  {
    v4u qs[6];
    UNROLL for (int i = 0; i < 6; ++i) qs[i] = ld16<SC1>(rw, t16, qso + i * 4096);
    v4u kf[6][4], vf[8][3];
    auto kfrag = [&](int s, int kt) { kf[s][kt] = ld16<SC1>(rw, fo2[s & 1] + kt * 256, kso[s >> 1]); };
    auto vfrag = [&](int v) { vf[v / 3][v % 3] = ld16<SC1>(rw, vv2[(v / 3) & 1] + (v % 3) * 1024, vso[v / 6]); };
    UNROLL for (int n = 0; n < 8; ++n) kfrag(n >> 2, n & 3);
    FENCE();
    UNROLL for (int n = 27; n < 36; ++n) wof[n] = wotr(n / 3, n % 3);
    FENCE();
    UNROLL for (int i = 0; i < 6; ++i) sts16(qw + i * 4096, qs[i]);
    FENCE();
    UNROLL for (int n = 0; n < 4; ++n) kfrag(2, n);
    bar();
    f4 sa[4][4];
    float m[4];
    auto rowmax = [&](int qt) {
      float a = mx2(mx2(sa[0][qt][0], sa[0][qt][1]), sa[0][qt][2]), b = mx2(mx2(sa[0][qt][3], sa[1][qt][0]), sa[1][qt][1]);
      a = mx2(mx2(a, sa[1][qt][2]), sa[1][qt][3]); b = mx2(mx2(b, sa[2][qt][0]), sa[2][qt][1]);
      a = mx2(mx2(a, sa[2][qt][2]), sa[2][qt][3]); b = mx2(mx2(b, sa[3][qt][0]), sa[3][qt][1]);
      m[qt] = mx2(mx2(mx2(a, sa[3][qt][2]), sa[3][qt][3]), b);
      opq(m[qt]);
    };
    {
      auto qfrag = [&](int n) { return lds16(qa[n >> 3] + ((n >> 2) & 1) * 4096 + (n & 3) * 256); };
      v4u ring[RING];
      UNROLL for (int n = 0; n < RING; ++n) ring[n] = qfrag(n);
      FENCE();
      UNROLL for (int n = 0; n < 24; ++n) {
        const int s = n >> 2, qt = n & 3;
        const v4u qb = ring[n % RING];
        mm<false>(sa[0][qt], kf[s][0], qb, false, s == 0);
        FENCE();
        if (n + RING < 24) ring[n % RING] = qfrag(n + RING);
        FENCE();
        mm<false>(sa[1][qt], kf[s][1], qb, false, s == 0);
        FENCE();
        if (s < 3) kfrag(s + 3, qt); else vfrag(n - 12);
        FENCE();
        mm<false>(sa[2][qt], kf[s][2], qb, false, s == 0);
        mm<false>(sa[3][qt], kf[s][3], qb, false, s == 0);
        FENCE();
        if (n == 23) { rowmax(0); rowmax(1); }
        FENCE();
      }
    }
    TS(8);
    vfrag(12);
    FENCE();
    rowmax(2);
    vfrag(13);
    FENCE();
    rowmax(3);
    FENCE();
    {
      auto a = __builtin_amdgcn_permlane16_swap(asu(m[0]), asu(m[1]), false, false);
      auto b = __builtin_amdgcn_permlane16_swap(asu(m[2]), asu(m[3]), false, false);
      const float p = mx2(asf(a[0]), asf(a[1])), q = mx2(asf(b[0]), asf(b[1]));
      auto c = __builtin_amdgcn_permlane32_swap(asu(p), asu(q), false, false);
      *LP(float, mxw) = mx2(asf(c[0]), asf(c[1]));
    }
    vfrag(14); vfrag(15);
    v2u rtv = ld8<SC1>(rw, rt_w1, 0);
    bar();
    f4 m4[4];
    UNROLL for (int qt = 0; qt < 4; ++qt) m4[qt] = ldsf4(mxa + qt * 256);
    const f4 mo4 = ldsf4(mxo);
    FENCE();
    UNROLL for (int qt = 0; qt < 4; ++qt) {
      const float mq = mx4(m4[qt]);
      UNROLL for (int u = 0; u < 2; ++u) {
        vfrag(16 + 2 * qt + u);
        FENCE();
        const f4 a = sa[2 * u][qt] - mq, b = sa[2 * u + 1][qt] - mq;
        sts16(pw + (u * 4 + qt) * 1024,
              v4u{pk(__builtin_amdgcn_exp2f(a[0]), __builtin_amdgcn_exp2f(a[1])), pk(__builtin_amdgcn_exp2f(a[2]), __builtin_amdgcn_exp2f(a[3])),
                  pk(__builtin_amdgcn_exp2f(b[0]), __builtin_amdgcn_exp2f(b[1])), pk(__builtin_amdgcn_exp2f(b[2]), __builtin_amdgcn_exp2f(b[3]))});
        FENCE();
      }
    }
    float mw = mx4(mo4);
    opq(mw);
    v4u ones = {0x3f803f80u, 0x3f803f80u, 0x3f803f80u, 0x3f803f80u};
    asm volatile("" : "+v"(ones));
    poll_from(rtv, rw, rt_w1, tag);
    FRESH_L16();
    bar();
    TS(9);
    f4 lsum;
    f4 oa[2][2][3];
    auto oepi = [&](int tp, int dt) { st16<SC1>(pair_swap(pk4(oa[tp][0][dt]), pk4(oa[tp][1][dt])), rw, oso[dt] + tp * 512); };
    auto pfrag = [&](int n) {
      return lds16(pa[(n & 15) >> 2] + ((n >> 1) & 1) * 4096 + (2 * (n >> 4) + (n & 1)) * 1024);
    };
    v4u ring[RING], lsf[2];
    UNROLL for (int n = 0; n < RING; ++n) ring[n] = pfrag(n);
    FENCE();
    UNROLL for (int n = 0; n < 32; ++n) {
      const int tp = n >> 4, s = (n & 15) >> 1, t = n & 1;
      const v4u pb = ring[n % RING];
      mm<false>(oa[tp][t][0], vf[s][0], pb, false, s == 0);
      FENCE();
      if (n + RING < 32) ring[n % RING] = pfrag(n + RING);
      FENCE();
      mm<false>(oa[tp][t][1], vf[s][1], pb, false, s == 0);
      FENCE();
      if ((n & 1) == 0 && n / 2 < W1_P) w1f[n / 2] = FRAG(n / 2, w1s);
      if (n == 19 || n == 23 || n == 27) oepi(0, (n - 19) / 4);
      if (n == 25) { asm volatile("" : "+v"(lsum)); st8<SC1>(v2u{asu(mw), asu(lsum[0])}, rw, mlo); }
      FENCE();
      mm<false>(oa[tp][t][2], vf[s][2], pb, false, s == 0);
      if ((n & 1) && n >= 5 && n < 21) mm<false>(lsum, ones, lsf[(n >> 1) & 1], true, n == 5);
      FENCE();
      if ((n & 1) && n < 17) lsf[(n >> 1) & 1] = lds16(lso + (n >> 1) * 4096);
      FENCE();
    }
    NOP8();
    UNROLL for (int dt = 0; dt < 3; ++dt) oepi(1, dt);
    TS(10);
  }
  FENCE();
  v2u gv[3], xres[3];
  const int go = vopq(WS_MOD + (bat * 2304 + 1536) * 8 + tid * 8);
  mod_request(gv, rw, go);
  const v2u rt2 = ld8<SC1>(rw, rt_w2, 0);
  UNROLL for (int n = W1_P; n < W1_B0; ++n) w1f[n] = FRAG(n, w1s);
  FENCE();
  vmwait<4 + W1_B0 - W1_P>();
  bar();
  if (w == 0) { st8<SC1>(tag, rw, fpb); fv = ld8<SC1>(rw, fl + 128, 0); }
  TS(11);
  FRESH_L16();
  const int xo = vopq(i16 * 768 + g * 8);
  UNROLL for (int n = W1_B0; n < W1_B; ++n) w1f[n] = FRAG(n, w1s);
  UNROLL for (int ft = 0; ft < 3; ++ft) xres[ft] = ld8<0>(rx, xo + ft * 32, (team * 64 + c_tt * 16) * 768 + (c_ch * 192 + w * 48) * 2);
  FENCE();
  int w2s[6], oca[3], ops[4];
  UNROLL for (int j = 0; j < 6; ++j) w2s[j] = sopq(WS_W2T + (e_cs * 4 + w) * 73728 + mod3(j / 2 + r3) * 24576 + (j & 1) * 12288);
  UNROLL for (int j = 0; j < 3; ++j) oca[j] = vopq(lb + L_OC + mod3(j + r3) * 4096 + lane * 16);
  UNROLL for (int s = 0; s < 4; ++s) ops[s] = sopq(WS_OP + (team * 8 + (w >> 1) * 4 + s) * 24576 + (w & 1) * 12288 + c_tt * 256);
  const int fo3 = vopq(g * 1024 + i16 * 16 + 8192);
  const int mla = vopq(WS_ML + (team * 8 + (w >> 1) * 4) * 512 + c_tt * 128 + i16 * 8);
  const int ocw = vopq(lb + L_OC + 3 * w * 1024 + lane * 16);
  const int gaa = vopq(lb + L_GATE + (c_ch * 192 + w * 48 + g * 4) * 4), gd = vopq(lb + L_GATE + tid * 4);
  const int xd = WS_X1 + team * 49152 + (c_ch * 24 + w * 6) * 1024 + c_tt * 256 + i16 * 16;
  const int xd0 = vopq(xd + ((g & 1) * 2 + (g >> 1)) * 1024), xd1 = vopq((g & 1) ? OOB : xd + (4 + (g >> 1)) * 1024);
  const int sto = vopq(g > 1 ? OOB : WS_ST + team * 4096 + (c_tt * 8 + c_ch * 4 + w) * 128 + lane * 4);
  const int tx = vopq(lane < 48 ? WS_X1 + team * 49152 + (c_ch * 24 + (lane >> 1)) * 1024 + c_tt * 256 + (lane & 1) * 128
                    : lane < 52 ? WS_ST + team * 4096 + (c_tt * 8 + c_ch * 4 + lane - 48) * 128 : OOB);
  FENCE();
  poll_from(rt2, rw, rt_w2, tag);
  mod_finish<false>(gv, rw, go, gd, gd + 1024, gd + 2048, gtag, tid, 1.f);
  if (w == 0) poll_from(fv, rw, fl + 128, tag);
  bar();
  TS(13);

  {
    FRESH_L16();
    v2u ml[4];
    UNROLL for (int s = 0; s < 4; ++s) ml[s] = ld8<SC1>(rw, mla + s * 512, 0);
    v4u op[3][4];
    UNROLL for (int i = 0; i < 3; ++i)
      UNROLL for (int s = 0; s < 4; ++s) op[i][s] = ld16<SC1>(rw, i < 2 ? fo2[i] : fo3, ops[s]);
    FENCE();
    float wt[4];
    {
      const float m = mx2(mx2(asf(ml[0].x), asf(ml[1].x)), mx2(asf(ml[2].x), asf(ml[3].x)));
      UNROLL for (int s = 0; s < 4; ++s) wt[s] = __builtin_amdgcn_exp2f(asf(ml[s].x) - m);
      const float inv = __builtin_amdgcn_rcpf((wt[0] * asf(ml[0].y) + wt[1] * asf(ml[1].y)) + (wt[2] * asf(ml[2].y) + wt[3] * asf(ml[3].y)));
      UNROLL for (int s = 0; s < 4; ++s) wt[s] *= inv;
    }
    FENCE();
    UNROLL for (int i = 0; i < 3; ++i) {
      if (i > 0) { UNROLL for (int n = W1_B + (i - 1) * (W1_C0 - W1_B) / 2; n < W1_B + i * (W1_C0 - W1_B) / 2; ++n) w1f[n] = FRAG(n, w1s); }
      FENCE();
      v4u o;
      UNROLL for (int e = 0; e < 4; ++e) {
        f2 a = f2{lo(op[i][0][e]), hi(op[i][0][e])} * wt[0];
        UNROLL for (int s = 1; s < 4; ++s) a += f2{lo(op[i][s][e]), hi(op[i][s][e])} * wt[s];
        o[e] = pk(a[0], a[1]);
      }
      sts16(ocw + i * 1024, o);
      FENCE();
    }
    bar();
    TS(27);
    v4u ob[12];
    UNROLL for (int s = 0; s < 12; ++s) ob[s] = lds16(oca[s >> 2] + (s & 3) * 1024);
    f4 ga[3], xf[3];
    UNROLL for (int ft = 0; ft < 3; ++ft) ga[ft] = ldsf4(gaa + ft * 64);
    FENCE();
    f4 acc[3];
    UNROLL for (int s = 0; s < 12; ++s) {
      mm<false>(acc[0], wof[s * 3], ob[s], false, s == 0);
      FENCE();
      if ((s & 1) == 0) {
        const int n = W1_C0 + s / 2;
        if (n < 39) w1f[n] = FRAG(n, w1s); else if (n - 39 < W2_C) w2f[n - 39] = FRAG(n - 39, w2s);
      }
      if (s >= 3 && s < 9 && (s & 1)) {
        const int ft = (s - 3) / 2;
        xf[ft] = f4{lo(xres[ft].x), hi(xres[ft].x), lo(xres[ft].y), hi(xres[ft].y)};
        asm volatile("" : "+v"(xf[ft]));
      }
      FENCE();
      mm<false>(acc[1], wof[s * 3 + 1], ob[s], false, s == 0);
      mm<false>(acc[2], wof[s * 3 + 2], ob[s], false, s == 0);
      FENCE();
    }
    NOP8();
    v2u xp[3];
    UNROLL for (int ft = 0; ft < 3; ++ft) xp[ft] = pk4(xf[ft] + ga[ft] * acc[ft]);
    st16<SC1>(pair_swap(xp[0], xp[1]), rw, xd0);
    st16<SC1>(pair_swap(xp[2], xp[2]), rw, xd1);
    FENCE();
    f2 s1 = {0.f, 0.f}, s2 = {0.f, 0.f};
    UNROLL for (int ft = 0; ft < 3; ++ft) {
      const f2 a = {lo(xp[ft].x), hi(xp[ft].x)}, b = {lo(xp[ft].y), hi(xp[ft].y)};
      s1 += a + b;
      s2 += a * a + b * b;
    }
    {
      auto a = __builtin_amdgcn_permlane16_swap(asu(s1[0] + s1[1]), asu(s2[0] + s2[1]), false, false);
      const float p = asf(a[0]) + asf(a[1]);
      auto b = __builtin_amdgcn_permlane32_swap(asu(p), asu(p), false, false);
      __builtin_amdgcn_raw_buffer_store_b32(asu(asf(b[0]) + asf(b[1])), rw, sto, 0, SC1);
    }
    TS(14);
  }
  FENCE();
  UNROLL for (int n = W2_C; n < W2_W0; ++n) w2f[n] = FRAG(n, w2s);
  FENCE();
  vmwait<W2_W0 - W2_C>();
  bar();
  if (w == 0) { st8<SC1>(tag, rw, fp + 256); fv = ld8<SC1>(rw, fl + 256, 0); }
  else if (K_TOUCH && w == 1) touch(lb + L_MODA + 256, rw, tx);
  TS(15);
  FRESH_L16();
  UNROLL for (int n = W2_W0; n < W2_W; ++n) w2f[n] = FRAG(n, w2s);
  FENCE();
  int xa[4], hso[3];
  UNROLL for (int j = 0; j < 3; ++j) {
    xa[j] = vopq(lb + L_ACT + mod3(j + r3) * 16384 + g * 1024 + i16 * 16);
    hso[j] = vopq(WS_HH + team * 196608 + (mem * 24 + w * 6 + 2 * j) * 1024 + pst);
  }
  xa[3] = vopq(lb + L_ACT + 49152 + i16 * 16);
  const int xw = vopq(lb + L_ACT + tid * 16);
  const int x1s = sopq(WS_X1 + team * 49152);
  const int sta = vopq(WS_ST + team * 4096 + w * 1024 + g * 256 + i16 * 4);
  const int stw = vopq(lb + L_STL + (w * 16 + i16) * 16), ste = vopq(lb + L_ACT + 49152 + (w * 16 + i16) * 16), kka = vopq(lb + L_STL + i16 * 16);
  FENCE();
  if (w == 0) poll_from(fv, rw, fl + 256, tag);
  if (w == 2 && blockIdx.x == 0) {
    v2u sv[4];
    UNROLL for (int i = 0; i < 4; ++i) sv[i] = ld8<SC1>(rw, WS_START + lane * 8 + i * 512, 0);
    UNROLL for (int i = 0; i < 4; ++i) poll_from(sv[i], rw, WS_START + lane * 8 + i * 512, tag);
    st8<SC1>(tag, rw, lane ? OOB : WS_EPOCH);
  }
  bar();
  TS(17);

  {
    FRESH_L16();
    u32 stv[4];
    UNROLL for (int e = 0; e < 4; ++e) stv[e] = __builtin_amdgcn_raw_buffer_load_b32(rw, sta + e * 64, 0, SC1);
    v4u xs[12];
    UNROLL for (int i = 0; i < 12; ++i) xs[i] = ld16<SC1>(rw, t16, x1s + i * 4096);
    FENCE();
    {
      const float mu = xg_sum(asf(stv[0]) + asf(stv[2])) * (1.f / 384.f);
      const float var = fmaxf(xg_sum(asf(stv[1]) + asf(stv[3])) * (1.f / 384.f) - mu * mu, 0.f) + 1e-5f;
      const float r = __builtin_amdgcn_rsqf(var), sg = var * r;
      const float mh = rbf(-mu), sh = rbf(sg);
      *LP(f4, stw) = f4{-2.30220819f * r, -0.10294324f * r * r * r, r, 0.f};
      sts16(ste, v4u{pk(mh, -mu - mh), pk(mh, sh), pk(sg - sh, sh), 0u});
    }
    FENCE();
    UNROLL for (int i = 0; i < 12; ++i) {
      sts16(xw + i * 4096, xs[i]);
      if (i >= 6 && W2_W + i - 6 < W2_D0) w2f[W2_W + i - 6] = FRAG(W2_W + i - 6, w2s);
      FENCE();
    }
    bar();
    TS(18);
    auto xfrag = [&](int s, int t) { return lds16(xa[s >> 2] + (s & 3) * 4096 + t * 256); };
    v4u xb[13];
    UNROLL for (int s = 0; s < 13; ++s) xb[s] = xfrag(s, 0);
    f2 kk[4];
    UNROLL for (int t = 0; t < 4; ++t) kk[t] = *LP(f2, kka + t * 256);
    FENCE();
    f4 acc[2];
    float y[4];
    v2u hp[2][3];
    auto stage = [&](int q, int j) {
      const int t = q / 3, ft = q % 3;
      const f4& z = acc[q & 1];
      if (j == 0) { UNROLL for (int e = 0; e < 4; ++e) y[e] = v_mul(z[e], z[e]); }
      if (j == 1) { UNROLL for (int e = 0; e < 4; ++e) y[e] = v_fma(y[e], kk[t][1], kk[t][0]); }
      if (j == 2) { UNROLL for (int e = 0; e < 4; ++e) y[e] = v_mul(y[e], z[e]); }
      if (j == 3 || j == 4) { UNROLL for (int e = 2 * j - 6; e < 2 * j - 4; ++e) y[e] = v_exp(y[e]); }
      if (j == 5) { UNROLL for (int e = 0; e < 4; ++e) y[e] = v_inc(y[e]); }
      if (j == 6 || j == 7) { UNROLL for (int e = 2 * j - 12; e < 2 * j - 10; ++e) y[e] = v_rcp(y[e]); }
      if (j == 8) { UNROLL for (int e = 0; e < 4; ++e) y[e] = v_mul(y[e], z[e]); }
      if (j == 9) hp[t & 1][ft] = v2u{v_pk(y[0], y[1]), v_pk(y[2], y[3])};
      if (j == 10 && (t & 1)) st16<SC1>(pair_swap(hp[0][ft], hp[1][ft]), rw, hso[ft] + (t >> 1) * 512);
    };
    UNROLL for (int q = 0; q < 12; ++q) {
      const int t = q / 3, ft = q % 3;
      UNROLL for (int s = 0; s < 13; ++s) {
        mm<false>(acc[q & 1], w1f[s * 3 + ft], xb[s], w1_in_v(s * 3 + ft), s == 0);
        FENCE();
        if (ft == 2 && t < 3) xb[s] = xfrag(s, t + 1);
        if (s < 3 && W2_D0 + 3 * q + s < W2_D) w2f[W2_D0 + 3 * q + s] = FRAG(W2_D0 + 3 * q + s, w2s);
        if (q > 1 && s < 2) stage(q - 2, 9 + s);
        if (q > 0 && s == 3) asm volatile("s_nop 1");
        if (q > 0 && s >= 4) stage(q - 1, s - 4);
        FENCE();
      }
    }
    stage(10, 9);
    FENCE();
    asm volatile("s_nop 1");
    FENCE();
    stage(10, 10);
    NOP8();
    UNROLL for (int j = 0; j < 10; ++j) stage(11, j);
    FENCE();
    asm volatile("s_nop 1");
    FENCE();
    stage(11, 10);
    TS(19);
  }
  FENCE();
  UNROLL for (int n = W2_D; n < W2_E0; ++n) w2f[n] = FRAG(n, w2s);
  FENCE();
  vmwait<W2_E0 - W2_D>();
  bar();
  if (w == 0) { st8<SC1>(tag, rw, fp + 384); fv = ld8<SC1>(rw, fl + 384, 0); }
  TS(20);
  FRESH_L16();
  UNROLL for (int n = W2_E0; n < W2_E; ++n) w2f[n] = FRAG(n, w2s);
  FENCE();
  v4u x1r[2];
  f4 gm[2][2];
  int ra[2], oo[2], hv[4], hs[3];
  UNROLL for (int i = 0; i < 2; ++i) {
    const int n = tid + 256 * i, nn = n < 384 ? n : 383, tok = nn / 12, c8 = nn % 12;
    x1r[i] = lds16(lb + L_ACT + (e_cs * 12 + c8) * 1024 + (e_th * 32 + tok) * 16);
    const float r = *LP(float, lb + L_STL + (e_th * 32 + tok) * 16 + 8);
    gm[i][0] = ldsf4(lb + L_GATE + 1536 + (e_cs * 96 + c8 * 8) * 4) * r;
    gm[i][1] = ldsf4(lb + L_GATE + 1536 + (e_cs * 96 + c8 * 8) * 4 + 16) * r;
    asm volatile("" : "+v"(gm[i][0]), "+v"(gm[i][1]), "+v"(x1r[i]));
    ra[i] = vopq(lb + L_RED + tok * 400 + c8 * 32);
    oo[i] = vopq(n < 384 ? (team * 64 + e_th * 32 + tok) * 768 + e_cs * 192 + c8 * 16 : OOB);
  }
  UNROLL for (int j = 0; j < 4; ++j) hv[j] = vopq(g * 1024 + i16 * 16 + j * 4096);
  UNROLL for (int j = 0; j < 3; ++j) hs[j] = sopq(WS_HH + team * 196608 + w * 49152 + e_th * 512 + mod3(j + r3) * 16384);
  const int rdw = vopq(lb + L_RED + w * 12800 + i16 * 400 + g * 16);
  const rsrc_t ro = mkrs(out, 2048 * 384 * 2);
  FENCE();
  if (w == 0) poll_from(fv, rw, fl + 384, tag);
  bar();
  TS(22);

  {
    v4u hb[12][2];
    UNROLL for (int j = 0; j < 4; ++j) hv[j] = vopq(hv[j]);
    FRESH_L16();
    auto hload = [&](int n) { hb[n >> 1][n & 1] = ld16<SC1>(rw, hv[(n >> 1) & 3] + (n & 1) * 256, hs[n >> 3]); };
    UNROLL for (int n = 0; n < 10; ++n) hload(n);
    FENCE();
    f4 acc[2][6];
    UNROLL for (int n = 0; n < 24; ++n) {
      const int s = n >> 1, t = n & 1;
      mm<false>(acc[t][0], w2f[s * 6], hb[s][t], w2_in_v(s * 6), s == 0);
      FENCE();
      if (n + 10 < 24) hload(n + 10);
      else if (W2_E + n - 14 < 72) w2f[W2_E + n - 14] = FRAG(W2_E + n - 14, w2s);
      FENCE();
      UNROLL for (int ft = 1; ft < 6; ++ft) {
        mm<false>(acc[t][ft], w2f[s * 6 + ft], hb[s][t], w2_in_v(s * 6 + ft), s == 0);
        if (n == 23 && ft >= 3) { FENCE(); *LP(f4, rdw + (ft - 3) * 64) = acc[0][ft - 3]; FENCE(); }
      }
      FENCE();
    }
    UNROLL for (int ft = 3; ft < 6; ++ft) *LP(f4, rdw + ft * 64) = acc[0][ft];
    FENCE();
    asm volatile("s_nop 3");
    FENCE();
    UNROLL for (int ft = 0; ft < 6; ++ft) { *LP(f4, rdw + 6400 + ft * 64) = acc[1][ft]; FENCE(); }
    bar();
    TS(23);
    UNROLL for (int i = 0; i < 2; ++i) {
      if (i == 0 || w < 2) {
        f4 pa_[4], pb_[4];
        UNROLL for (int q = 0; q < 4; ++q) { pa_[q] = ldsf4(ra[i] + q * 12800); pb_[q] = ldsf4(ra[i] + q * 12800 + 16); }
        FENCE();
        const f4 a = (pa_[0] + pa_[1]) + (pa_[2] + pa_[3]), b = (pb_[0] + pb_[1]) + (pb_[2] + pb_[3]);
        const v4u xr = x1r[i];
        const f4 g0 = gm[i][0], g1 = gm[i][1];
        const v4u o = {pk(lo(xr.x) + g0[0] * a[0], hi(xr.x) + g0[1] * a[1]), pk(lo(xr.y) + g0[2] * a[2], hi(xr.y) + g0[3] * a[3]),
                       pk(lo(xr.z) + g1[0] * b[0], hi(xr.z) + g1[1] * b[1]), pk(lo(xr.w) + g1[2] * b[2], hi(xr.w) + g1[3] * b[3])};
        st16<SC1>(o, ro, oo[i]);
      }
    }
    TS(24);
  }
  asm volatile("dit_code_end:");
}

}

extern "C" void kernel_launch(void* const* d_in, const int* in_sizes, int n_in, void* d_out, int out_size, void* d_ws,
                              size_t ws_size, hipStream_t stream) {
  (void)in_sizes; (void)n_in; (void)out_size; (void)ws_size;
  (void)hipFuncSetAttribute((const void*)dit::dit_kernel, hipFuncAttributeMaxDynamicSharedMemorySize, dit::L_BYTES);
  dit::dit_kernel<<<256, 256, dit::L_BYTES, stream>>>((const u16*)d_in[0], (const u16*)d_in[1], (const u16*)d_in[2], (const u16*)d_in[3],
                                                      (const u16*)d_in[4], (const u16*)d_in[5], (const u16*)d_in[6], (u16*)d_out,
                                                      (char*)d_ws);
}
```
